```python
import torch

def _softmax(x, axis=-1):
    return torch.softmax(x, dim=axis)
def _gelu(x):
    return torch.nn.functional.gelu(x, approximate="tanh")
def _silu(x):
    return torch.nn.functional.silu(x)
def _sigmoid(x):
    return torch.sigmoid(x)
def _layernorm(x, axis=-1, eps=1e-5):
    m = x.mean(dim=axis, keepdim=True)
    v = x.var(dim=axis, keepdim=True, unbiased=False)
    return (x - m) / torch.sqrt(v + eps)
def _scatter_add(tgt, idx, src, axis=0):
    return tgt.clone().index_add_(axis, idx, src)
def _segment_sum(src, seg, n):
    out = torch.zeros((n,) + src.shape[1:], dtype=src.dtype)
    return out.index_add_(0, seg, src)
def _pad(x, pw):
    flat = tuple(v for pair in reversed(pw) for v in pair)
    return torch.nn.functional.pad(x, flat)

kernel_name = 'gated_mix__mha_block__dense_activation_mish__prompt'

def setup_inputs():

    _w = (lambda a: (lambda t: t.to(torch.bfloat16) if t.is_floating_point() else t)(torch.as_tensor(a)))
    def _A_setup():
        B, S, D, H = 1, 2048, 512, 32
        return {
            "x": _w((lambda *s: torch.randn(*s, dtype=torch.bfloat16) * 0.05)(B, S, D)),
            "wqkv": _w((lambda *s: torch.randn(*s, dtype=torch.bfloat16) * 0.8)(D, 3 * D)),
            "wo": _w((lambda *s: torch.randn(*s, dtype=torch.bfloat16) * 0.05)(D, D)),
        }
    _A_in = _A_setup()

    def _B_setup():
        B, S, D, F = 1, 2048, 512, 1792
        return {
            "x": _w((lambda *s: torch.randn(*s, dtype=torch.bfloat16) * 0.05)(B, S, D)),
            "w1": _w((lambda *s: torch.randn(*s, dtype=torch.bfloat16) * 12.8)(D, F) * (D ** -0.5)),
            "b1": _w((lambda *s: torch.randn(*s, dtype=torch.bfloat16) * 0.2)(F)),
            "w2": _w((lambda *s: torch.randn(*s, dtype=torch.bfloat16) * 0.1)(F, D) * (F ** -0.5)),
        }
    _B_in = _B_setup()

    return {
        "A_x": _A_in["x"],
        "A_wqkv": _A_in["wqkv"],
        "A_wo": _A_in["wo"],
        "B_x": _B_in["x"],
        "B_w1": _B_in["w1"],
        "B_b1": _B_in["b1"],
        "B_w2": _B_in["w2"],
        "gate_w": _w((lambda *s: torch.randn(*s, dtype=torch.bfloat16) * 0.05)(512) * 16.0),
    }

def reference(A_x, A_wqkv, A_wo, B_x, B_w1, B_b1, B_w2, gate_w):
    def _A_ref(x, wqkv, wo):
        B, S, D = x.shape
        H = 32
        Dh = D // H
        qkv = (lambda x, *d: x.permute(*d))((x @ wqkv).reshape(B, S, 3, H, Dh), 2, 0, 3, 1, 4)
        q, k, v = qkv[0], qkv[1], qkv[2]
        scores = torch.einsum("bhqd,bhkd->bhqk", q, k) * (Dh ** -0.5)
        p = _softmax(scores, axis=-1)
        o = torch.einsum("bhqk,bhkd->bhqd", p, v)
        return (lambda x, *d: x.permute(*d))(o, 0, 2, 1, 3).reshape(B, S, D) @ wo

    def _B_ref(x, w1, b1, w2):
        h = torch.einsum("bsd,df->bsf", x, w1) + b1
        sp = torch.log1p(torch.exp(-torch.abs(h))) + torch.where(h > 0, h, torch.zeros_like(h))
        act = h * torch.tanh(sp)
        return torch.einsum("bsf,fd->bsd", act, w2)

    x = A_x
    g = _sigmoid((x * gate_w).sum(-1, keepdims=True))
    a = _A_ref(x, A_wqkv, A_wo)
    b = _B_ref(x, B_w1, B_b1, B_w2)
    return g * a + (1 - g) * b
```

```cpp
#include <hip/hip_runtime.h>
#include <hip/hip_bf16.h>
#include <stdint.h>
#include <type_traits>

typedef unsigned short u16;
using bf16x8 = __attribute__((ext_vector_type(8))) short;
using s16x4  = __attribute__((ext_vector_type(4))) short;
using f32x16 = __attribute__((ext_vector_type(16))) float;
using u32x4  = __attribute__((ext_vector_type(4))) unsigned;
using u32x2  = __attribute__((ext_vector_type(2))) unsigned;
#define DI __device__ __forceinline__
#define LAS __attribute__((address_space(3)))
#define MFMA(a, b, c) __builtin_amdgcn_mfma_f32_32x32x16_bf16((a), (b), (c), 0, 0, 0)
typedef LAS const char* lds_cptr;

constexpr int SEQ = 2048, DM = 512, FF = 1792, NH = 32;
constexpr size_t WS_Q = 0, WS_K = 2u << 20, WS_V = 4u << 20, WS_O = 6u << 20, WS_H = 8u << 20, WS_G = 16u << 20;
constexpr float QSCALE = 0.25f * 1.4426950408889634f;
constexpr float LOG2E = 1.4426950408889634f;

typedef float f32x2 __attribute__((ext_vector_type(2)));
typedef __bf16 bf16x2 __attribute__((ext_vector_type(2)));
DI unsigned cvtpk(float lo, float hi) { return __builtin_bit_cast(unsigned, __builtin_convertvector((f32x2){lo, hi}, bf16x2)); }
#define STAMP(kern, i)
DI u32x4 pack8(const f32x16& a, int s) { return (u32x4){cvtpk(a[8*s], a[8*s+1]), cvtpk(a[8*s+2], a[8*s+3]), cvtpk(a[8*s+4], a[8*s+5]), cvtpk(a[8*s+6], a[8*s+7])}; }
DI void glds16(const void* sbase, unsigned voff, unsigned lds_dst) {
    unsigned sv; asm volatile("s_mov_b32 %0, m0\n\ts_mov_b32 m0, %3\n\ts_nop 0\n\tglobal_load_lds_dwordx4 %1, %2\n\ts_mov_b32 m0, %0" : "=&s"(sv) : "v"(voff), "s"(sbase), "s"(lds_dst) : "memory"); }
typedef short v4i16_t __attribute__((ext_vector_type(4)));
DI s16x4 trd(lds_cptr p) { return __builtin_bit_cast(s16x4, __builtin_amdgcn_ds_read_tr16_b64_v4i16((LAS v4i16_t*)p)); }
DI bf16x8 trfrag(lds_cptr p, int second) { s16x4 a = trd(p), b = trd(p + second); return __builtin_shufflevector(a, b, 0, 1, 2, 3, 4, 5, 6, 7); }
DI bf16x8 ldfrag(lds_cptr p) { return *(LAS const bf16x8*)p; }
#define WAITV(n) asm volatile("s_waitcnt vmcnt(" #n ")" ::: "memory")
#define BARRIER() asm volatile("s_waitcnt lgkmcnt(0)\n\ts_barrier" ::: "memory")
DI float mish(float h) {
    const float u = __builtin_amdgcn_exp2f(__builtin_fminf(h * LOG2E, 40.f));
    const float n = u * (u + 2.f);
    return h * n * __builtin_amdgcn_rcpf(n + 2.f);
}

constexpr int K1_STAGE = 22 * 1024, K1_NS = 7, K1_LDS = K1_STAGE * K1_NS;
__global__ __launch_bounds__(512, 2) void k1_proj(const char* __restrict__ x, const char* __restrict__ wqkv, const char* __restrict__ w1,
                                                  const unsigned* __restrict__ b1, const char* __restrict__ gw, char* __restrict__ ws) {
    extern __shared__ __attribute__((aligned(16))) char lds[];
    const int tid = threadIdx.x, lane = tid & 63, w = __builtin_amdgcn_readfirstlane(tid >> 6);
    const int b = blockIdx.x, xcd = b & 7, bi = b >> 3;
    const int r = (xcd & 1) * 8 + (bi & 7), c = (xcd >> 1) * 4 + (bi >> 3);
    const int ch = c < 14 ? c : 13;
    const int rot = (2 * r + 5 * c) & 15;
    const unsigned l0 = (unsigned)(uintptr_t)lds;
    unsigned long long ts0 = __builtin_amdgcn_s_memrealtime(), ts1 = 0;
    const unsigned xoff = (unsigned)((16 * w + (lane >> 2)) * 1024 + (((lane & 3) ^ ((lane >> 4) & 3)) * 16));
    const char* xb = x + (size_t)r * 128 * 1024;
    const int t1 = w >> 1, kh = w & 1;
    const char* wb1; unsigned pitch1;
    if (t1 < 3) { wb1 = wqkv + (t1 * 512 + 32 * c) * 2; pitch1 = 3072; } else { wb1 = w1 + (128 * ch) * 2; pitch1 = 3584; }
    const unsigned woff1 = (unsigned)((kh * 16 + (lane >> 2)) * pitch1 + (lane & 3) * 16);
    const char* wb2 = w1 + (128 * ch + 32 * (1 + t1)) * 2;
    const unsigned woff2 = (unsigned)((kh * 16 + (lane >> 2)) * 3584 + (lane & 3) * 16);
    auto issue = [&](int i, unsigned slot) {
        const int kc = (i + rot) & 15;
        glds16(xb + kc * 64, xoff, slot + w * 1024);
        glds16(wb1 + (size_t)kc * 32 * pitch1, woff1, slot + 8192 + w * 1024);
        if (w < 6) glds16(wb2 + (size_t)kc * 32 * 3584, woff2, slot + 16384 + w * 1024);
    };
#pragma unroll
    for (int i = 0; i < 6; ++i) issue(i, l0 + i * K1_STAGE);

    unsigned long long ts4 = __builtin_amdgcn_s_memrealtime();
    const int tt = w & 3, half = w >> 2, hi = lane >> 5;
    f32x16 acc0 = {}, acc1 = {}, acc2, acc3;
    {
        typedef unsigned u32x16 __attribute__((ext_vector_type(16)));
        const unsigned* bp = b1 + (128 * ch + 64 * half) / 2;
        u32x16 bA, bB;
        asm volatile("s_load_dwordx16 %0, %2, 0x0\n\ts_load_dwordx16 %1, %2, 0x40\n\ts_waitcnt lgkmcnt(0)" : "=&s"(bA), "=&s"(bB) : "s"(bp) : "memory");
#pragma unroll
        for (int g = 0; g < 4; ++g)
#pragma unroll
            for (int j = 0; j < 4; ++j) {
                const unsigned a0 = hi ? bA[4 * g + 2 + (j >> 1)] : bA[4 * g + (j >> 1)];
                const unsigned a1 = hi ? bB[4 * g + 2 + (j >> 1)] : bB[4 * g + (j >> 1)];
                acc2[4 * g + j] = __uint_as_float((j & 1) ? (a0 & 0xffff0000u) : (a0 << 16));
                acc3[4 * g + j] = __uint_as_float((j & 1) ? (a1 & 0xffff0000u) : (a1 << 16));
            }
    }
    unsigned long long ts5 = __builtin_amdgcn_s_memrealtime(), ts6 = 0;
    const int xrow = 32 * tt + (lane & 31), sw = (lane >> 2) & 3;
    const unsigned xr0 = xrow * 64 + ((hi ^ sw) * 16), xr1 = xr0 ^ 32;
    const unsigned wr = 8192 + (8 * hi + ((lane & 15) >> 2)) * 64 + ((lane >> 4) & 1) * 32 + (lane & 3) * 8;
    auto mainloop = [&](auto HALF) {
#pragma unroll
        for (int i = 0; i < 16; ++i) {
            const int rem = (15 - i) < 5 ? (15 - i) : 5;
            if (HALF.value == 0 || w < 6) asm volatile("s_waitcnt vmcnt(%0)" :: "i"(3 * rem) : "memory");
            else asm volatile("s_waitcnt vmcnt(%0)" :: "i"(2 * rem) : "memory");
            BARRIER();
            if (i == 0) ts1 = __builtin_amdgcn_s_memrealtime(); if (i == 8) ts6 = __builtin_amdgcn_s_memrealtime();
            if (i + 6 < 16) issue(i + 6, l0 + ((i + 6) % K1_NS) * K1_STAGE);
            lds_cptr sp = (lds_cptr)lds + (i % K1_NS) * K1_STAGE;
#pragma unroll
            for (int ks = 0; ks < 2; ++ks) {
                const bf16x8 xf = ldfrag(sp + (ks ? xr1 : xr0));
                lds_cptr wp = sp + wr + ks * 1024;
                if (HALF.value == 0) {
                    acc0 = MFMA(trfrag(wp + 0 * 2048, 256), xf, acc0);
                    acc1 = MFMA(trfrag(wp + 1 * 2048, 256), xf, acc1);
                    acc2 = MFMA(trfrag(wp + 3 * 2048, 256), xf, acc2);
                    acc3 = MFMA(trfrag(wp + 4 * 2048, 256), xf, acc3);
                } else {
                    acc0 = MFMA(xf, trfrag(wp + 2 * 2048, 256), acc0);
                    acc2 = MFMA(trfrag(wp + 5 * 2048, 256), xf, acc2);
                    acc3 = MFMA(trfrag(wp + 6 * 2048, 256), xf, acc3);
                }
            }
        }
    };
    if (half == 0) mainloop(std::integral_constant<int, 0>{}); else mainloop(std::integral_constant<int, 1>{});
    unsigned long long ts2 = __builtin_amdgcn_s_memrealtime();
    const int tile = r * 4 + tt;
    if (half == 0) {
#pragma unroll
        for (int i = 0; i < 16; ++i) acc0[i] *= QSCALE;
#pragma unroll
        for (int s = 0; s < 2; ++s) {
            *(u32x4*)(ws + WS_Q + ((size_t)((2 * c + s) * 64 + tile) * 64 + lane) * 16) = pack8(acc0, s);
            *(u32x4*)(ws + WS_K + ((size_t)((2 * c + s) * 64 + tile) * 64 + lane) * 16) = pack8(acc1, s);
        }
    } else {
        const int head = 2 * c + ((lane >> 4) & 1);
#pragma unroll
        for (int s = 0; s < 2; ++s)
            *(u32x4*)(ws + WS_V + (size_t)(head * 64 + tile) * 1024 + s * 512 + hi * 256 + (lane & 15) * 16) = pack8(acc0, s);
    }
    *(u32x4*)(ws + WS_O + ((size_t)(b * 8 + w) * 64 + lane) * 16) = (u32x4){~0u, ~0u, ~0u, ~0u};
    if (c < 14) {
#pragma unroll
        for (int i = 0; i < 16; ++i) { acc2[i] = mish(acc2[i]); acc3[i] = mish(acc3[i]); }
        const int fs = 8 * c + 4 * half;
#pragma unroll
        for (int s = 0; s < 2; ++s) {
            *(u32x4*)(ws + WS_H + ((size_t)(tile * 112 + fs + s) * 64 + lane) * 16) = pack8(acc2, s);
            *(u32x4*)(ws + WS_H + ((size_t)(tile * 112 + fs + 2 + s) * 64 + lane) * 16) = pack8(acc3, s);
        }
    } else if (c == 15) {
        const u32x4 gv = *(const u32x4*)(gw + lane * 16);
        float gf[8];
#pragma unroll
        for (int j = 0; j < 4; ++j) { gf[2 * j] = __uint_as_float(gv[j] << 16); gf[2 * j + 1] = __uint_as_float(gv[j] & 0xffff0000u); }
        float mine = 0.f;
#pragma unroll
        for (int rr = 0; rr < 16; ++rr) {
            const u32x4 xv = *(const u32x4*)(xb + (size_t)(16 * w + rr) * 1024 + lane * 16);
            float s = 0.f;
#pragma unroll
            for (int j = 0; j < 4; ++j) { s = __builtin_fmaf(__uint_as_float(xv[j] << 16), gf[2 * j], s); s = __builtin_fmaf(__uint_as_float(xv[j] & 0xffff0000u), gf[2 * j + 1], s); }
#pragma unroll
            for (int m = 32; m >= 1; m >>= 1) s += __shfl_xor(s, m);
            if (lane == rr) mine = s;
        }
        if (lane < 16) ((float*)(ws + WS_G))[r * 128 + 16 * w + lane] = __builtin_amdgcn_rcpf(1.f + __builtin_amdgcn_exp2f(-mine * LOG2E));
    }
}

constexpr int A_K = 0, A_V = 65536, A_ONES = 131072, K2_LDS = 131072 + 2048;
DI float halfmax(float m) { auto rr = __builtin_amdgcn_permlane32_swap(__float_as_uint(m), __float_as_uint(m), false, false); return __builtin_fmaxf(__uint_as_float(rr[0]), __uint_as_float(rr[1])); }
DI float max16(const f32x16& p) {
    float a = __builtin_fmaxf(__builtin_fmaxf(p[0], p[1]), p[2]), b = __builtin_fmaxf(__builtin_fmaxf(p[3], p[4]), p[5]);
    a = __builtin_fmaxf(__builtin_fmaxf(a, p[6]), p[7]); b = __builtin_fmaxf(__builtin_fmaxf(b, p[8]), p[9]);
    a = __builtin_fmaxf(__builtin_fmaxf(a, p[10]), p[11]); b = __builtin_fmaxf(__builtin_fmaxf(b, p[12]), p[13]);
    a = __builtin_fmaxf(__builtin_fmaxf(a, p[14]), p[15]); return __builtin_fmaxf(a, b); }

__global__ __launch_bounds__(512, 2) void k2_attn(char* __restrict__ ws) {
    extern __shared__ __attribute__((aligned(16))) char lds[];
    const int tid = threadIdx.x, lane = tid & 63, w = __builtin_amdgcn_readfirstlane(tid >> 6), hi = lane >> 5;
    const int b = blockIdx.x, head = (b & 7) * 4 + ((b >> 3) & 3), r = b >> 5;
    const unsigned l0 = (unsigned)(uintptr_t)lds;
    const char* kb = ws + WS_K + (size_t)head * 65536; const char* vb = ws + WS_V + (size_t)head * 65536;
    const int tile = r * 8 + w;
    bf16x8 qf; float g;
    asm volatile("global_load_dwordx4 %0, %1, %2" : "=v"(qf) : "v"(lane * 16), "s"(ws + WS_Q + (size_t)(head * 64 + tile) * 1024) : "memory");
    asm volatile("global_load_dword %0, %1, %2" : "=v"(g) : "v"((lane & 31) * 4), "s"(ws + WS_G + tile * 128) : "memory");
#pragma unroll
    for (int j = 0; j < 8; ++j) { glds16(kb + (j * 8 + w) * 1024, lane * 16, l0 + A_K + (j * 8 + w) * 1024); glds16(vb + (j * 8 + w) * 1024, lane * 16, l0 + A_V + (j * 8 + w) * 1024); }
    if (tid < 128) *(u32x4*)(lds + A_ONES + tid * 16) = (u32x4){0x3f803f80u, 0x3f803f80u, 0x3f803f80u, 0x3f803f80u};
    asm volatile("s_waitcnt vmcnt(14) lgkmcnt(0)\n\ts_barrier" : "+v"(qf), "+v"(g) :: "memory");
    lds_cptr kp = (lds_cptr)lds + A_K + lane * 16;
    const bool vl = (lane & 16) == 0;
    unsigned va = (vl ? A_V : A_ONES) + hi * 256 + (lane & 15) * 16; const unsigned vstep = vl ? 2048 : 0;
    f32x16 zero = {};
    f32x16 s0 = MFMA(ldfrag(kp), qf, zero);
    const float m = halfmax(max16(s0));
    f32x16 negm;
#pragma unroll
    for (int i = 0; i < 16; ++i) negm[i] = -m;
    f32x16 o = {};
    for (int t = 0; t < 64; t += 2) {
        if (t == 8) { WAITV(0); BARRIER(); }
#pragma unroll
        for (int u = 0; u < 2; ++u) {
            f32x16 s = MFMA(ldfrag(kp + (t + u) * 1024), qf, negm);
            lds_cptr vp = (lds_cptr)lds + va + u * 1024;
            const bf16x8 v0 = ldfrag(vp), v1 = ldfrag(vp + 512);
#pragma unroll
            for (int i = 0; i < 16; ++i) s[i] = __builtin_amdgcn_exp2f(s[i]);
            o = MFMA(v0, __builtin_bit_cast(bf16x8, pack8(s, 0)), o);
            o = MFMA(v1, __builtin_bit_cast(bf16x8, pack8(s, 1)), o);
        }
        va += vstep;
    }
    float big = __builtin_fabsf(o[8]);
#pragma unroll
    for (int i = 0; i < 8; ++i) big = __builtin_fmaxf(big, __builtin_fabsf(o[i]) * 0x1p-60f);
    if (__builtin_expect(__any(!(big < 0x1p60f) || !(o[8] > 0x1p-20f)), 0)) {
        float mr = -INFINITY; o = zero; va = (vl ? A_V : A_ONES) + hi * 256 + (lane & 15) * 16;
        for (int t = 0; t < 64; ++t) {
            f32x16 s = MFMA(ldfrag(kp + t * 1024), qf, zero);
            const float mn = __builtin_fmaxf(mr, halfmax(max16(s)));
            const float al = __builtin_amdgcn_exp2f(mr - mn); mr = mn;
#pragma unroll
            for (int i = 0; i < 16; ++i) { o[i] *= al; s[i] = __builtin_amdgcn_exp2f(s[i] - mn); }
            lds_cptr vp = (lds_cptr)lds + va;
            o = MFMA(ldfrag(vp), __builtin_bit_cast(bf16x8, pack8(s, 0)), o);
            o = MFMA(ldfrag(vp + 512), __builtin_bit_cast(bf16x8, pack8(s, 1)), o);
            va += vstep >> 1;
        }
    }
    const float sc = g * __builtin_amdgcn_rcpf(o[8]);
#pragma unroll
    for (int i = 0; i < 8; ++i) o[i] *= sc;
    *(u32x4*)(ws + WS_O + ((size_t)(tile * 32 + head) * 64 + lane) * 16) = pack8(o, 0);
}

constexpr int K3_LDS = 65536;
__global__ __launch_bounds__(256, 2) void k3_mlp(const char* __restrict__ w2, const char* __restrict__ wo, char* __restrict__ ws, char* __restrict__ out) {
    extern __shared__ __attribute__((aligned(16))) char lds[];
    const int tid = threadIdx.x, lane = tid & 63, m = __builtin_amdgcn_readfirstlane(tid >> 6), hi = lane >> 5;
    const int b = blockIdx.x, rg = (b & 7) * 4 + ((b >> 3) & 3), ct = b >> 5;
    const int tA = (rg >> 2) * 8 + (rg & 3), tB = tA + 4;
    const unsigned ring = (unsigned)(uintptr_t)lds + m * 8192;
    const unsigned woff = (lane >> 2) * 1024 + (lane & 3) * 16;
    const char* w2b = w2 + (size_t)(28 * m) * 16 * 1024 + ct * 128;
    const char* hA = ws + WS_H + ((size_t)(tA * 112 + 28 * m) * 64) * 16;
    const char* hB = ws + WS_H + ((size_t)(tB * 112 + 28 * m) * 64) * 16;
    const unsigned lo = lane * 16;
    u32x4 ha[4], hb[4];
#define LDH(dst, base, off) asm volatile("global_load_dwordx4 %0, %1, %2" : "=v"(dst) : "v"(lo), "s"((base) + (off)) : "memory")
#define ISSUE(ks, sl) do { glds16(w2b + (size_t)(ks) * 16384, woff, ring + (sl) * 2048); glds16(w2b + (size_t)(ks) * 16384 + 64, woff, ring + (sl) * 2048 + 1024); \
        LDH(ha[sl], hA, (ks) * 1024); LDH(hb[sl], hB, (ks) * 1024); } while (0)
    float gA, gB;
    asm volatile("global_load_dword %0, %1, %2" : "=v"(gA) : "v"((lane & 31) * 4), "s"(ws + WS_G + tA * 128) : "memory");
    asm volatile("global_load_dword %0, %1, %2" : "=v"(gB) : "v"((lane & 31) * 4), "s"(ws + WS_G + tB * 128) : "memory");
    ISSUE(0, 0); ISSUE(1, 1); ISSUE(2, 2); ISSUE(3, 3);
    f32x16 a00 = {}, a01 = {}, a10 = {}, a11 = {};
    lds_cptr wr = (lds_cptr)lds + m * 8192 + (4 * hi + ((lane & 15) >> 2)) * 64 + ((lane >> 4) & 1) * 32 + (lane & 3) * 8;
#define STEP(sl, WAIT) do { asm volatile("s_waitcnt vmcnt(" #WAIT ")" : "+v"(ha[sl]), "+v"(hb[sl]) :: "memory"); \
        const bf16x8 f0 = trfrag(wr + (sl) * 2048, 512), f1 = trfrag(wr + (sl) * 2048 + 1024, 512); \
        const bf16x8 xa = __builtin_bit_cast(bf16x8, ha[sl]), xb = __builtin_bit_cast(bf16x8, hb[sl]); \
        a00 = MFMA(f0, xa, a00); a01 = MFMA(f0, xb, a01); a10 = MFMA(f1, xa, a10); a11 = MFMA(f1, xb, a11); } while (0)
#define LGK0() asm volatile("s_waitcnt lgkmcnt(0)" ::: "memory")
    for (int k = 4; k < 28; k += 4) {
        STEP(0, 12); LGK0(); ISSUE(k + 0, 0);
        STEP(1, 12); LGK0(); ISSUE(k + 1, 1);
        STEP(2, 12); LGK0(); ISSUE(k + 2, 2);
        STEP(3, 12); LGK0(); ISSUE(k + 3, 3);
    }
    STEP(0, 12); STEP(1, 8); STEP(2, 4); STEP(3, 0);
    asm volatile("" : "+v"(gA), "+v"(gB));
#pragma unroll
    for (int i = 0; i < 16; ++i) { a00[i] *= 1.f - gA; a10[i] *= 1.f - gA; a01[i] *= 1.f - gB; a11[i] *= 1.f - gB; }
    const char* wob = wo + (size_t)(8 * m) * 16 * 1024 + ct * 128;
    const char* oA = ws + WS_O + ((size_t)(tA * 32 + 8 * m) * 64) * 16;
    const char* oB = ws + WS_O + ((size_t)(tB * 32 + 8 * m) * 64) * 16;
#define ISSUE2(ks, sl) do { glds16(wob + (size_t)(ks) * 16384, woff, ring + (sl) * 2048); glds16(wob + (size_t)(ks) * 16384 + 64, woff, ring + (sl) * 2048 + 1024); \
        LDH(ha[sl], oA, (ks) * 1024); LDH(hb[sl], oB, (ks) * 1024); } while (0)
    asm volatile("s_waitcnt lgkmcnt(0)" ::: "memory");
    ISSUE2(0, 0); ISSUE2(1, 1); ISSUE2(2, 2); ISSUE2(3, 3);
    STEP(0, 12); asm volatile("s_waitcnt lgkmcnt(0)" ::: "memory"); ISSUE2(4, 0);
    STEP(1, 12); asm volatile("s_waitcnt lgkmcnt(0)" ::: "memory"); ISSUE2(5, 1);
    STEP(2, 12); asm volatile("s_waitcnt lgkmcnt(0)" ::: "memory"); ISSUE2(6, 2);
    STEP(3, 12); asm volatile("s_waitcnt lgkmcnt(0)" ::: "memory"); ISSUE2(7, 3);
    STEP(0, 12); STEP(1, 8); STEP(2, 4); STEP(3, 0);
    BARRIER();
    typedef float f32x4 __attribute__((ext_vector_type(4)));
    auto put = [&](const f32x16& a, int tileIdx) {
        if (tileIdx != m) {
#pragma unroll
            for (int q = 0; q < 4; ++q) *(f32x4*)(lds + ((tileIdx * 4 + m) * 4 + q) * 1024 + lane * 16) = (f32x4){a[4 * q], a[4 * q + 1], a[4 * q + 2], a[4 * q + 3]};
        } };
    put(a00, 0); put(a01, 1); put(a10, 2); put(a11, 3);
    BARRIER();
    f32x16 acc = m == 0 ? a00 : m == 1 ? a01 : m == 2 ? a10 : a11;
#pragma unroll
    for (int o = 1; o < 4; ++o) {
        const int src = (m + o) & 3;
#pragma unroll
        for (int q = 0; q < 4; ++q) { const f32x4 v = *(const f32x4*)(lds + ((m * 4 + src) * 4 + q) * 1024 + lane * 16); acc[4 * q] += v[0]; acc[4 * q + 1] += v[1]; acc[4 * q + 2] += v[2]; acc[4 * q + 3] += v[3]; }
    }
    const int tok = ((m & 1) ? tB : tA) * 32 + (lane & 31);
    char* op = out + (size_t)tok * 1024 + ct * 128 + (m >> 1) * 64 + hi * 16;
#pragma unroll
    for (int p = 0; p < 2; ++p) {
        unsigned a0 = cvtpk(acc[8 * p], acc[8 * p + 1]), a1 = cvtpk(acc[8 * p + 2], acc[8 * p + 3]);
        unsigned b0 = cvtpk(acc[8 * p + 4], acc[8 * p + 5]), b1 = cvtpk(acc[8 * p + 6], acc[8 * p + 7]);
        auto r0 = __builtin_amdgcn_permlane32_swap(a0, b0, false, false);
        auto r1 = __builtin_amdgcn_permlane32_swap(a1, b1, false, false);
        *(u32x4*)(op + p * 32) = (u32x4){r0[0], r1[0], r0[1], r1[1]};
    }
}

extern "C" void kernel_launch(void* const* d_in, const int* in_sizes, int n_in, void* d_out, int out_size, void* d_ws, size_t ws_size, hipStream_t stream) {
    d_ws = (char*)d_ws + (((size_t)(ws_size - (17u<<20) - 0)) & ~(size_t)0x1fffff);
    (void)hipFuncSetAttribute((const void*)k1_proj, hipFuncAttributeMaxDynamicSharedMemorySize, K1_LDS);
    (void)hipFuncSetAttribute((const void*)k2_attn, hipFuncAttributeMaxDynamicSharedMemorySize, K2_LDS);
    (void)hipFuncSetAttribute((const void*)k3_mlp, hipFuncAttributeMaxDynamicSharedMemorySize, K3_LDS);
    k1_proj<<<256, 512, K1_LDS, stream>>>((const char*)d_in[0], (const char*)d_in[1], (const char*)d_in[4], (const unsigned*)d_in[5], (const char*)d_in[7], (char*)d_ws);
    k2_attn<<<256, 512, K2_LDS, stream>>>((char*)d_ws);
    k3_mlp<<<256, 256, K3_LDS, stream>>>((const char*)d_in[6], (const char*)d_in[2], (char*)d_ws, (char*)d_out);
}
```

```cpp
#define SEL 0
#include <hip/hip_runtime.h>
#include <hip/hip_bf16.h>
#include <stdint.h>
#include <type_traits>

typedef unsigned short u16;
using bf16x8 = __attribute__((ext_vector_type(8))) short;
using s16x4  = __attribute__((ext_vector_type(4))) short;
using f32x16 = __attribute__((ext_vector_type(16))) float;
using u32x4  = __attribute__((ext_vector_type(4))) unsigned;
using u32x2  = __attribute__((ext_vector_type(2))) unsigned;
#define DI __device__ __forceinline__
#define LAS __attribute__((address_space(3)))
#define MFMA(a, b, c) __builtin_amdgcn_mfma_f32_32x32x16_bf16((a), (b), (c), 0, 0, 0)
typedef LAS const char* lds_cptr;

constexpr int SEQ = 2048, DM = 512, FF = 1792, NH = 32;
constexpr size_t WS_Q = 0, WS_K = 2u << 20, WS_V = 4u << 20, WS_O = 6u << 20, WS_H = 8u << 20, WS_G = 16u << 20;
constexpr float QSCALE = 0.25f * 1.4426950408889634f;
constexpr float LOG2E = 1.4426950408889634f;

typedef float f32x2 __attribute__((ext_vector_type(2)));
typedef __bf16 bf16x2 __attribute__((ext_vector_type(2)));
DI unsigned cvtpk(float lo, float hi) { return __builtin_bit_cast(unsigned, __builtin_convertvector((f32x2){lo, hi}, bf16x2)); }
constexpr size_t WS_ST2 = 48u << 20;
#define RT() __builtin_amdgcn_s_memrealtime()
#define KBEGIN() const unsigned long long kb_t = RT(), kb_c = __builtin_amdgcn_s_memtime()
#define KEND(k) do { asm volatile("s_waitcnt vmcnt(0)" ::: "memory"); if ((threadIdx.x & 63) == 0) { unsigned long long* p_ = (unsigned long long*)(ws + WS_ST2) + (((k) * 256 + blockIdx.x) * 8 + (threadIdx.x >> 6)) * 4; p_[0] = kb_t; p_[1] = RT(); p_[2] = __builtin_amdgcn_s_memtime() - kb_c; } } while (0)
#define STAMP(kern, i)
DI u32x4 pack8(const f32x16& a, int s) { return (u32x4){cvtpk(a[8*s], a[8*s+1]), cvtpk(a[8*s+2], a[8*s+3]), cvtpk(a[8*s+4], a[8*s+5]), cvtpk(a[8*s+6], a[8*s+7])}; }
DI void glds16(const void* sbase, unsigned voff, unsigned lds_dst) {
    unsigned sv; asm volatile("s_mov_b32 %0, m0\n\ts_mov_b32 m0, %3\n\ts_nop 0\n\tglobal_load_lds_dwordx4 %1, %2\n\ts_mov_b32 m0, %0" : "=&s"(sv) : "v"(voff), "s"(sbase), "s"(lds_dst) : "memory"); }
typedef short v4i16_t __attribute__((ext_vector_type(4)));
DI s16x4 trd(lds_cptr p) { return __builtin_bit_cast(s16x4, __builtin_amdgcn_ds_read_tr16_b64_v4i16((LAS v4i16_t*)p)); }
DI bf16x8 trfrag(lds_cptr p, int second) { s16x4 a = trd(p), b = trd(p + second); return __builtin_shufflevector(a, b, 0, 1, 2, 3, 4, 5, 6, 7); }
DI bf16x8 ldfrag(lds_cptr p) { return *(LAS const bf16x8*)p; }
#define WAITV(n) asm volatile("s_waitcnt vmcnt(" #n ")" ::: "memory")
#define BARRIER() asm volatile("s_waitcnt lgkmcnt(0)\n\ts_barrier" ::: "memory")
DI float mish(float h) {
    const float u = __builtin_amdgcn_exp2f(__builtin_fminf(h * LOG2E, 40.f));
    const float n = u * (u + 2.f);
    return h * n * __builtin_amdgcn_rcpf(n + 2.f);
}

constexpr int K1_STAGE = 22 * 1024, K1_NS = 7, K1_LDS = K1_STAGE * K1_NS;
__global__ __launch_bounds__(512, 2) void k1_proj(const char* __restrict__ x, const char* __restrict__ wqkv, const char* __restrict__ w1,
                                                  const unsigned* __restrict__ b1, const char* __restrict__ gw, char* __restrict__ ws) {
    extern __shared__ __attribute__((aligned(16))) char lds[];
    KBEGIN();
    const int tid = threadIdx.x, lane = tid & 63, w = __builtin_amdgcn_readfirstlane(tid >> 6);
    const int b = blockIdx.x, xcd = b & 7, bi = b >> 3;
    const int r = (xcd & 1) * 8 + (bi & 7), c = (xcd >> 1) * 4 + (bi >> 3);
    const int ch = c < 14 ? c : 13;
    const int rot = (2 * r + 5 * c) & 15;
    const unsigned l0 = (unsigned)(uintptr_t)lds;
    unsigned long long ts0 = __builtin_amdgcn_s_memrealtime(), ts1 = 0;
    const unsigned xoff = (unsigned)((16 * w + (lane >> 2)) * 1024 + (((lane & 3) ^ ((lane >> 4) & 3)) * 16));
    const char* xb = x + (size_t)r * 128 * 1024;
    const int t1 = w >> 1, kh = w & 1;
    const char* wb1; unsigned pitch1;
    if (t1 < 3) { wb1 = wqkv + (t1 * 512 + 32 * c) * 2; pitch1 = 3072; } else { wb1 = w1 + (128 * ch) * 2; pitch1 = 3584; }
    const unsigned woff1 = (unsigned)((kh * 16 + (lane >> 2)) * pitch1 + (lane & 3) * 16);
    const char* wb2 = w1 + (128 * ch + 32 * (1 + t1)) * 2;
    const unsigned woff2 = (unsigned)((kh * 16 + (lane >> 2)) * 3584 + (lane & 3) * 16);
    auto issue = [&](int i, unsigned slot) {
        const int kc = (i + rot) & 15;
        glds16(xb + kc * 64, xoff, slot + w * 1024);
        glds16(wb1 + (size_t)kc * 32 * pitch1, woff1, slot + 8192 + w * 1024);
        if (w < 6) glds16(wb2 + (size_t)kc * 32 * 3584, woff2, slot + 16384 + w * 1024);
    };
#pragma unroll
    for (int i = 0; i < 6; ++i) issue(i, l0 + i * K1_STAGE);

    unsigned long long ts4 = __builtin_amdgcn_s_memrealtime();
    const int tt = w & 3, half = w >> 2, hi = lane >> 5;
    f32x16 acc0 = {}, acc1 = {}, acc2, acc3;
    {
        typedef unsigned u32x16 __attribute__((ext_vector_type(16)));
        const unsigned* bp = b1 + (128 * ch + 64 * half) / 2;
        u32x16 bA, bB;
        asm volatile("s_load_dwordx16 %0, %2, 0x0\n\ts_load_dwordx16 %1, %2, 0x40\n\ts_waitcnt lgkmcnt(0)" : "=&s"(bA), "=&s"(bB) : "s"(bp) : "memory");
#pragma unroll
        for (int g = 0; g < 4; ++g)
#pragma unroll
            for (int j = 0; j < 4; ++j) {
                const unsigned a0 = hi ? bA[4 * g + 2 + (j >> 1)] : bA[4 * g + (j >> 1)];
                const unsigned a1 = hi ? bB[4 * g + 2 + (j >> 1)] : bB[4 * g + (j >> 1)];
                acc2[4 * g + j] = __uint_as_float((j & 1) ? (a0 & 0xffff0000u) : (a0 << 16));
                acc3[4 * g + j] = __uint_as_float((j & 1) ? (a1 & 0xffff0000u) : (a1 << 16));
            }
    }
    unsigned long long ts5 = __builtin_amdgcn_s_memrealtime(), ts6 = 0;
    const int xrow = 32 * tt + (lane & 31), sw = (lane >> 2) & 3;
    const unsigned xr0 = xrow * 64 + ((hi ^ sw) * 16), xr1 = xr0 ^ 32;
    const unsigned wr = 8192 + (8 * hi + ((lane & 15) >> 2)) * 64 + ((lane >> 4) & 1) * 32 + (lane & 3) * 8;
    auto mainloop = [&](auto HALF) {
#pragma unroll
        for (int i = 0; i < 16; ++i) {
            const int rem = (15 - i) < 5 ? (15 - i) : 5;
            if (HALF.value == 0 || w < 6) asm volatile("s_waitcnt vmcnt(%0)" :: "i"(3 * rem) : "memory");
            else asm volatile("s_waitcnt vmcnt(%0)" :: "i"(2 * rem) : "memory");
            BARRIER();
            if (i == 0) ts1 = __builtin_amdgcn_s_memrealtime(); if (i == 8) ts6 = __builtin_amdgcn_s_memrealtime();
            if (i + 6 < 16) issue(i + 6, l0 + ((i + 6) % K1_NS) * K1_STAGE);
            lds_cptr sp = (lds_cptr)lds + (i % K1_NS) * K1_STAGE;
#pragma unroll
            for (int ks = 0; ks < 2; ++ks) {
                const bf16x8 xf = ldfrag(sp + (ks ? xr1 : xr0));
                lds_cptr wp = sp + wr + ks * 1024;
                if (HALF.value == 0) {
                    acc0 = MFMA(trfrag(wp + 0 * 2048, 256), xf, acc0);
                    acc1 = MFMA(trfrag(wp + 1 * 2048, 256), xf, acc1);
                    acc2 = MFMA(trfrag(wp + 3 * 2048, 256), xf, acc2);
                    acc3 = MFMA(trfrag(wp + 4 * 2048, 256), xf, acc3);
                } else {
                    acc0 = MFMA(xf, trfrag(wp + 2 * 2048, 256), acc0);
                    acc2 = MFMA(trfrag(wp + 5 * 2048, 256), xf, acc2);
                    acc3 = MFMA(trfrag(wp + 6 * 2048, 256), xf, acc3);
                }
            }
        }
    };
    if (half == 0) mainloop(std::integral_constant<int, 0>{}); else mainloop(std::integral_constant<int, 1>{});
    unsigned long long ts2 = __builtin_amdgcn_s_memrealtime();
    const int tile = r * 4 + tt;
    if (half == 0) {
#pragma unroll
        for (int i = 0; i < 16; ++i) acc0[i] *= QSCALE;
#pragma unroll
        for (int s = 0; s < 2; ++s) {
            *(u32x4*)(ws + WS_Q + ((size_t)((2 * c + s) * 64 + tile) * 64 + lane) * 16) = pack8(acc0, s);
            *(u32x4*)(ws + WS_K + ((size_t)((2 * c + s) * 64 + tile) * 64 + lane) * 16) = pack8(acc1, s);
        }
    } else {
        const int head = 2 * c + ((lane >> 4) & 1);
#pragma unroll
        for (int s = 0; s < 2; ++s)
            *(u32x4*)(ws + WS_V + (size_t)(head * 64 + tile) * 1024 + s * 512 + hi * 256 + (lane & 15) * 16) = pack8(acc0, s);
    }
    *(u32x4*)(ws + WS_O + ((size_t)(b * 8 + w) * 64 + lane) * 16) = (u32x4){~0u, ~0u, ~0u, ~0u};
    if (c < 14) {
#pragma unroll
        for (int i = 0; i < 16; ++i) { acc2[i] = mish(acc2[i]); acc3[i] = mish(acc3[i]); }
        const int fs = 8 * c + 4 * half;
#pragma unroll
        for (int s = 0; s < 2; ++s) {
            *(u32x4*)(ws + WS_H + ((size_t)(tile * 112 + fs + s) * 64 + lane) * 16) = pack8(acc2, s);
            *(u32x4*)(ws + WS_H + ((size_t)(tile * 112 + fs + 2 + s) * 64 + lane) * 16) = pack8(acc3, s);
        }
    } else if (c == 15) {
        const u32x4 gv = *(const u32x4*)(gw + lane * 16);
        float gf[8];
#pragma unroll
        for (int j = 0; j < 4; ++j) { gf[2 * j] = __uint_as_float(gv[j] << 16); gf[2 * j + 1] = __uint_as_float(gv[j] & 0xffff0000u); }
        float mine = 0.f;
#pragma unroll
        for (int rr = 0; rr < 16; ++rr) {
            const u32x4 xv = *(const u32x4*)(xb + (size_t)(16 * w + rr) * 1024 + lane * 16);
            float s = 0.f;
#pragma unroll
            for (int j = 0; j < 4; ++j) { s = __builtin_fmaf(__uint_as_float(xv[j] << 16), gf[2 * j], s); s = __builtin_fmaf(__uint_as_float(xv[j] & 0xffff0000u), gf[2 * j + 1], s); }
#pragma unroll
            for (int m = 32; m >= 1; m >>= 1) s += __shfl_xor(s, m);
            if (lane == rr) mine = s;
        }
        if (lane < 16) ((float*)(ws + WS_G))[r * 128 + 16 * w + lane] = __builtin_amdgcn_rcpf(1.f + __builtin_amdgcn_exp2f(-mine * LOG2E));
    }
    KEND(0);
}

constexpr int A_K = 0, A_V = 65536, A_ONES = 131072, K2_LDS = 131072 + 2048;
DI float halfmax(float m) { auto rr = __builtin_amdgcn_permlane32_swap(__float_as_uint(m), __float_as_uint(m), false, false); return __builtin_fmaxf(__uint_as_float(rr[0]), __uint_as_float(rr[1])); }
DI float max16(const f32x16& p) {
    float a = __builtin_fmaxf(__builtin_fmaxf(p[0], p[1]), p[2]), b = __builtin_fmaxf(__builtin_fmaxf(p[3], p[4]), p[5]);
    a = __builtin_fmaxf(__builtin_fmaxf(a, p[6]), p[7]); b = __builtin_fmaxf(__builtin_fmaxf(b, p[8]), p[9]);
    a = __builtin_fmaxf(__builtin_fmaxf(a, p[10]), p[11]); b = __builtin_fmaxf(__builtin_fmaxf(b, p[12]), p[13]);
    a = __builtin_fmaxf(__builtin_fmaxf(a, p[14]), p[15]); return __builtin_fmaxf(a, b); }

__global__ __launch_bounds__(512, 2) void k2_attn(char* __restrict__ ws) {
    extern __shared__ __attribute__((aligned(16))) char lds[];
    KBEGIN();
    const int tid = threadIdx.x, lane = tid & 63, w = __builtin_amdgcn_readfirstlane(tid >> 6), hi = lane >> 5;
    const int b = blockIdx.x, head = (b & 7) * 4 + ((b >> 3) & 3), r = b >> 5;
    const unsigned l0 = (unsigned)(uintptr_t)lds;
    const char* kb = ws + WS_K + (size_t)head * 65536; const char* vb = ws + WS_V + (size_t)head * 65536;
    const int tile = r * 8 + w;
    bf16x8 qf; float g;
    asm volatile("global_load_dwordx4 %0, %1, %2" : "=v"(qf) : "v"(lane * 16), "s"(ws + WS_Q + (size_t)(head * 64 + tile) * 1024) : "memory");
    asm volatile("global_load_dword %0, %1, %2" : "=v"(g) : "v"((lane & 31) * 4), "s"(ws + WS_G + tile * 128) : "memory");
#pragma unroll
    for (int j = 0; j < 8; ++j) { glds16(kb + (j * 8 + w) * 1024, lane * 16, l0 + A_K + (j * 8 + w) * 1024); glds16(vb + (j * 8 + w) * 1024, lane * 16, l0 + A_V + (j * 8 + w) * 1024); }
    if (tid < 128) *(u32x4*)(lds + A_ONES + tid * 16) = (u32x4){0x3f803f80u, 0x3f803f80u, 0x3f803f80u, 0x3f803f80u};
    asm volatile("s_waitcnt vmcnt(14) lgkmcnt(0)\n\ts_barrier" : "+v"(qf), "+v"(g) :: "memory");
    lds_cptr kp = (lds_cptr)lds + A_K + lane * 16;
    const bool vl = (lane & 16) == 0;
    unsigned va = (vl ? A_V : A_ONES) + hi * 256 + (lane & 15) * 16; const unsigned vstep = vl ? 2048 : 0;
    f32x16 zero = {};
    f32x16 s0 = MFMA(ldfrag(kp), qf, zero);
    const float m = halfmax(max16(s0));
    f32x16 negm;
#pragma unroll
    for (int i = 0; i < 16; ++i) negm[i] = -m;
    f32x16 o = {};
    for (int t = 0; t < 64; t += 2) {
        if (t == 8) { WAITV(0); BARRIER(); }
#pragma unroll
        for (int u = 0; u < 2; ++u) {
            f32x16 s = MFMA(ldfrag(kp + (t + u) * 1024), qf, negm);
            lds_cptr vp = (lds_cptr)lds + va + u * 1024;
            const bf16x8 v0 = ldfrag(vp), v1 = ldfrag(vp + 512);
#pragma unroll
            for (int i = 0; i < 16; ++i) s[i] = __builtin_amdgcn_exp2f(s[i]);
            o = MFMA(v0, __builtin_bit_cast(bf16x8, pack8(s, 0)), o);
            o = MFMA(v1, __builtin_bit_cast(bf16x8, pack8(s, 1)), o);
        }
        va += vstep;
    }
    float big = __builtin_fabsf(o[8]);
#pragma unroll
    for (int i = 0; i < 8; ++i) big = __builtin_fmaxf(big, __builtin_fabsf(o[i]) * 0x1p-60f);
    if (__builtin_expect(__any(!(big < 0x1p60f) || !(o[8] > 0x1p-20f)), 0)) {
        float mr = -INFINITY; o = zero; va = (vl ? A_V : A_ONES) + hi * 256 + (lane & 15) * 16;
        for (int t = 0; t < 64; ++t) {
            f32x16 s = MFMA(ldfrag(kp + t * 1024), qf, zero);
            const float mn = __builtin_fmaxf(mr, halfmax(max16(s)));
            const float al = __builtin_amdgcn_exp2f(mr - mn); mr = mn;
#pragma unroll
            for (int i = 0; i < 16; ++i) { o[i] *= al; s[i] = __builtin_amdgcn_exp2f(s[i] - mn); }
            lds_cptr vp = (lds_cptr)lds + va;
            o = MFMA(ldfrag(vp), __builtin_bit_cast(bf16x8, pack8(s, 0)), o);
            o = MFMA(ldfrag(vp + 512), __builtin_bit_cast(bf16x8, pack8(s, 1)), o);
            va += vstep >> 1;
        }
    }
    const float sc = g * __builtin_amdgcn_rcpf(o[8]);
#pragma unroll
    for (int i = 0; i < 8; ++i) o[i] *= sc;
    *(u32x4*)(ws + WS_O + ((size_t)(tile * 32 + head) * 64 + lane) * 16) = pack8(o, 0);
    KEND(1);
}

constexpr int K3_LDS = 65536;
__global__ __launch_bounds__(256, 2) void k3_mlp(const char* __restrict__ w2, const char* __restrict__ wo, char* __restrict__ ws, char* __restrict__ out) {
    extern __shared__ __attribute__((aligned(16))) char lds[];
    KBEGIN();
    const int tid = threadIdx.x, lane = tid & 63, m = __builtin_amdgcn_readfirstlane(tid >> 6), hi = lane >> 5;
    const int b = blockIdx.x, rg = (b & 7) * 4 + ((b >> 3) & 3), ct = b >> 5;
    const int tA = (rg >> 2) * 8 + (rg & 3), tB = tA + 4;
    const unsigned ring = (unsigned)(uintptr_t)lds + m * 8192;
    const unsigned woff = (lane >> 2) * 1024 + (lane & 3) * 16;
    const char* w2b = w2 + (size_t)(28 * m) * 16 * 1024 + ct * 128;
    const char* hA = ws + WS_H + ((size_t)(tA * 112 + 28 * m) * 64) * 16;
    const char* hB = ws + WS_H + ((size_t)(tB * 112 + 28 * m) * 64) * 16;
    const unsigned lo = lane * 16;
    u32x4 ha[4], hb[4];
#define LDH(dst, base, off) asm volatile("global_load_dwordx4 %0, %1, %2" : "=v"(dst) : "v"(lo), "s"((base) + (off)) : "memory")
#define ISSUE(ks, sl) do { glds16(w2b + (size_t)(ks) * 16384, woff, ring + (sl) * 2048); glds16(w2b + (size_t)(ks) * 16384 + 64, woff, ring + (sl) * 2048 + 1024); \
        LDH(ha[sl], hA, (ks) * 1024); LDH(hb[sl], hB, (ks) * 1024); } while (0)
    float gA, gB;
    asm volatile("global_load_dword %0, %1, %2" : "=v"(gA) : "v"((lane & 31) * 4), "s"(ws + WS_G + tA * 128) : "memory");
    asm volatile("global_load_dword %0, %1, %2" : "=v"(gB) : "v"((lane & 31) * 4), "s"(ws + WS_G + tB * 128) : "memory");
    ISSUE(0, 0); ISSUE(1, 1); ISSUE(2, 2); ISSUE(3, 3);
    f32x16 a00 = {}, a01 = {}, a10 = {}, a11 = {};
    lds_cptr wr = (lds_cptr)lds + m * 8192 + (4 * hi + ((lane & 15) >> 2)) * 64 + ((lane >> 4) & 1) * 32 + (lane & 3) * 8;
#define STEP(sl, WAIT) do { asm volatile("s_waitcnt vmcnt(" #WAIT ")" : "+v"(ha[sl]), "+v"(hb[sl]) :: "memory"); \
        const bf16x8 f0 = trfrag(wr + (sl) * 2048, 512), f1 = trfrag(wr + (sl) * 2048 + 1024, 512); \
        const bf16x8 xa = __builtin_bit_cast(bf16x8, ha[sl]), xb = __builtin_bit_cast(bf16x8, hb[sl]); \
        a00 = MFMA(f0, xa, a00); a01 = MFMA(f0, xb, a01); a10 = MFMA(f1, xa, a10); a11 = MFMA(f1, xb, a11); } while (0)
#define LGK0() asm volatile("s_waitcnt lgkmcnt(0)" ::: "memory")
    for (int k = 4; k < 28; k += 4) {
        STEP(0, 12); LGK0(); ISSUE(k + 0, 0);
        STEP(1, 12); LGK0(); ISSUE(k + 1, 1);
        STEP(2, 12); LGK0(); ISSUE(k + 2, 2);
        STEP(3, 12); LGK0(); ISSUE(k + 3, 3);
    }
    STEP(0, 12); STEP(1, 8); STEP(2, 4); STEP(3, 0);
    asm volatile("" : "+v"(gA), "+v"(gB));
#pragma unroll
    for (int i = 0; i < 16; ++i) { a00[i] *= 1.f - gA; a10[i] *= 1.f - gA; a01[i] *= 1.f - gB; a11[i] *= 1.f - gB; }
    const char* wob = wo + (size_t)(8 * m) * 16 * 1024 + ct * 128;
    const char* oA = ws + WS_O + ((size_t)(tA * 32 + 8 * m) * 64) * 16;
    const char* oB = ws + WS_O + ((size_t)(tB * 32 + 8 * m) * 64) * 16;
#define ISSUE2(ks, sl) do { glds16(wob + (size_t)(ks) * 16384, woff, ring + (sl) * 2048); glds16(wob + (size_t)(ks) * 16384 + 64, woff, ring + (sl) * 2048 + 1024); \
        LDH(ha[sl], oA, (ks) * 1024); LDH(hb[sl], oB, (ks) * 1024); } while (0)
    asm volatile("s_waitcnt lgkmcnt(0)" ::: "memory");
    ISSUE2(0, 0); ISSUE2(1, 1); ISSUE2(2, 2); ISSUE2(3, 3);
    STEP(0, 12); asm volatile("s_waitcnt lgkmcnt(0)" ::: "memory"); ISSUE2(4, 0);
    STEP(1, 12); asm volatile("s_waitcnt lgkmcnt(0)" ::: "memory"); ISSUE2(5, 1);
    STEP(2, 12); asm volatile("s_waitcnt lgkmcnt(0)" ::: "memory"); ISSUE2(6, 2);
    STEP(3, 12); asm volatile("s_waitcnt lgkmcnt(0)" ::: "memory"); ISSUE2(7, 3);
    STEP(0, 12); STEP(1, 8); STEP(2, 4); STEP(3, 0);
    BARRIER();
    typedef float f32x4 __attribute__((ext_vector_type(4)));
    auto put = [&](const f32x16& a, int tileIdx) {
        if (tileIdx != m) {
#pragma unroll
            for (int q = 0; q < 4; ++q) *(f32x4*)(lds + ((tileIdx * 4 + m) * 4 + q) * 1024 + lane * 16) = (f32x4){a[4 * q], a[4 * q + 1], a[4 * q + 2], a[4 * q + 3]};
        } };
    put(a00, 0); put(a01, 1); put(a10, 2); put(a11, 3);
    BARRIER();
    f32x16 acc = m == 0 ? a00 : m == 1 ? a01 : m == 2 ? a10 : a11;
#pragma unroll
    for (int o = 1; o < 4; ++o) {
        const int src = (m + o) & 3;
#pragma unroll
        for (int q = 0; q < 4; ++q) { const f32x4 v = *(const f32x4*)(lds + ((m * 4 + src) * 4 + q) * 1024 + lane * 16); acc[4 * q] += v[0]; acc[4 * q + 1] += v[1]; acc[4 * q + 2] += v[2]; acc[4 * q + 3] += v[3]; }
    }
    const int tok = ((m & 1) ? tB : tA) * 32 + (lane & 31);
    char* op = out + (size_t)tok * 1024 + ct * 128 + (m >> 1) * 64 + hi * 16;
#pragma unroll
    for (int p = 0; p < 2; ++p) {
        unsigned a0 = cvtpk(acc[8 * p], acc[8 * p + 1]), a1 = cvtpk(acc[8 * p + 2], acc[8 * p + 3]);
        unsigned b0 = cvtpk(acc[8 * p + 4], acc[8 * p + 5]), b1 = cvtpk(acc[8 * p + 6], acc[8 * p + 7]);
        auto r0 = __builtin_amdgcn_permlane32_swap(a0, b0, false, false);
        auto r1 = __builtin_amdgcn_permlane32_swap(a1, b1, false, false);
        *(u32x4*)(op + p * 32) = (u32x4){r0[0], r1[0], r0[1], r1[1]};
    }
    KEND(2);
}

__global__ void reporter(char* ws) {
    const unsigned long long t0 = RT();
    const unsigned long long* p = (const unsigned long long*)(ws + WS_ST2);
    __shared__ unsigned long long lo[3], hi[3];
    if (threadIdx.x < 3) { lo[threadIdx.x] = ~0ull; hi[threadIdx.x] = 0; }
    __syncthreads();
    for (int k = 0; k < 3; ++k) { const int nw = k == 2 ? 4 : 8; for (int w = 0; w < nw; ++w) { const unsigned long long* q = p + ((k * 256 + threadIdx.x) * 8 + w) * 4; atomicMin(&lo[k], q[0]); atomicMax(&hi[k], q[1]); } }
    __syncthreads();
    unsigned long long X = 0;
    if (SEL == 1) X = hi[0] - lo[0];
    if (SEL == 2) X = 4 * (lo[1] - hi[0]);
    if (SEL == 3) X = hi[1] - lo[1];
    if (SEL == 4) X = 4 * (lo[2] - hi[1]);
    if (SEL == 5) X = hi[2] - lo[2];
    if (SEL == 6) { const unsigned long long* q = p + (1 * 256 * 8) * 4; X = q[2] * 100 / (q[1] - q[0]); }
    if (SEL == 7) X = 4 * (t0 - hi[2]);
    if (X > 20000) X = 20000;
    while (RT() < t0 + 200 + X) __builtin_amdgcn_s_sleep(1);
}
extern "C" void kernel_launch(void* const* d_in, const int* in_sizes, int n_in, void* d_out, int out_size, void* d_ws, size_t ws_size, hipStream_t stream) {
    (void)hipFuncSetAttribute((const void*)k1_proj, hipFuncAttributeMaxDynamicSharedMemorySize, K1_LDS);
    (void)hipFuncSetAttribute((const void*)k2_attn, hipFuncAttributeMaxDynamicSharedMemorySize, K2_LDS);
    (void)hipFuncSetAttribute((const void*)k3_mlp, hipFuncAttributeMaxDynamicSharedMemorySize, K3_LDS);
    k1_proj<<<256, 512, K1_LDS, stream>>>((const char*)d_in[0], (const char*)d_in[1], (const char*)d_in[4], (const unsigned*)d_in[5], (const char*)d_in[7], (char*)d_ws);
    k2_attn<<<256, 512, K2_LDS, stream>>>((char*)d_ws);
    k3_mlp<<<256, 256, K3_LDS, stream>>>((const char*)d_in[6], (const char*)d_in[2], (char*)d_ws, (char*)d_out);
    reporter<<<1,256,0,stream>>>((char*)d_ws);
}
```

```cpp
#define SEL 0
#include <hip/hip_runtime.h>
#include <hip/hip_bf16.h>
#include <stdint.h>
#include <type_traits>

typedef unsigned short u16;
using bf16x8 = __attribute__((ext_vector_type(8))) short;
using s16x4  = __attribute__((ext_vector_type(4))) short;
using f32x16 = __attribute__((ext_vector_type(16))) float;
using u32x4  = __attribute__((ext_vector_type(4))) unsigned;
using u32x2  = __attribute__((ext_vector_type(2))) unsigned;
#define DI __device__ __forceinline__
#define LAS __attribute__((address_space(3)))
#define MFMA(a, b, c) __builtin_amdgcn_mfma_f32_32x32x16_bf16((a), (b), (c), 0, 0, 0)
typedef LAS const char* lds_cptr;

constexpr int SEQ = 2048, DM = 512, FF = 1792, NH = 32;
constexpr size_t WS_Q = 0, WS_K = 2u << 20, WS_V = 4u << 20, WS_O = 6u << 20, WS_H = 8u << 20, WS_G = 16u << 20;
constexpr float QSCALE = 0.25f * 1.4426950408889634f;
constexpr float LOG2E = 1.4426950408889634f;

typedef float f32x2 __attribute__((ext_vector_type(2)));
typedef __bf16 bf16x2 __attribute__((ext_vector_type(2)));
DI unsigned cvtpk(float lo, float hi) { return __builtin_bit_cast(unsigned, __builtin_convertvector((f32x2){lo, hi}, bf16x2)); }
constexpr size_t WS_ST2 = 48u << 20;
#define RT() __builtin_amdgcn_s_memrealtime()
#define KBEGIN() const unsigned long long kb_t = RT(), kb_c = __builtin_amdgcn_s_memtime()
#define KEND(k) do { asm volatile("s_waitcnt vmcnt(0)" ::: "memory"); if ((threadIdx.x & 63) == 0) { unsigned long long* p_ = (unsigned long long*)(ws + WS_ST2) + (((k) * 256 + blockIdx.x) * 8 + (threadIdx.x >> 6)) * 4; p_[0] = kb_t; p_[1] = RT(); p_[2] = __builtin_amdgcn_s_memtime() - kb_c; } } while (0)
#define STAMP(kern, i)
DI u32x4 pack8(const f32x16& a, int s) { return (u32x4){cvtpk(a[8*s], a[8*s+1]), cvtpk(a[8*s+2], a[8*s+3]), cvtpk(a[8*s+4], a[8*s+5]), cvtpk(a[8*s+6], a[8*s+7])}; }
DI void glds16(const void* sbase, unsigned voff, unsigned lds_dst) {
    unsigned sv; asm volatile("s_mov_b32 %0, m0\n\ts_mov_b32 m0, %3\n\ts_nop 0\n\tglobal_load_lds_dwordx4 %1, %2\n\ts_mov_b32 m0, %0" : "=&s"(sv) : "v"(voff), "s"(sbase), "s"(lds_dst) : "memory"); }
typedef short v4i16_t __attribute__((ext_vector_type(4)));
DI s16x4 trd(lds_cptr p) { return __builtin_bit_cast(s16x4, __builtin_amdgcn_ds_read_tr16_b64_v4i16((LAS v4i16_t*)p)); }
DI bf16x8 trfrag(lds_cptr p, int second) { s16x4 a = trd(p), b = trd(p + second); return __builtin_shufflevector(a, b, 0, 1, 2, 3, 4, 5, 6, 7); }
DI bf16x8 ldfrag(lds_cptr p) { return *(LAS const bf16x8*)p; }
#define WAITV(n) asm volatile("s_waitcnt vmcnt(" #n ")" ::: "memory")
#define BARRIER() asm volatile("s_waitcnt lgkmcnt(0)\n\ts_barrier" ::: "memory")
DI float mish(float h) {
    const float u = __builtin_amdgcn_exp2f(__builtin_fminf(h * LOG2E, 40.f));
    const float n = u * (u + 2.f);
    return h * n * __builtin_amdgcn_rcpf(n + 2.f);
}

constexpr int K1_STAGE = 22 * 1024, K1_NS = 7, K1_LDS = K1_STAGE * K1_NS;
__global__ __launch_bounds__(512, 2) void k1_proj(const char* __restrict__ x, const char* __restrict__ wqkv, const char* __restrict__ w1,
                                                  const unsigned* __restrict__ b1, const char* __restrict__ gw, char* __restrict__ ws) {
    extern __shared__ __attribute__((aligned(16))) char lds[];
    KBEGIN();
    const int tid = threadIdx.x, lane = tid & 63, w = __builtin_amdgcn_readfirstlane(tid >> 6);
    const int b = blockIdx.x, xcd = b & 7, bi = b >> 3;
    const int r = (xcd & 1) * 8 + (bi & 7), c = (xcd >> 1) * 4 + (bi >> 3);
    const int ch = c < 14 ? c : 13;
    const int rot = (2 * r + 5 * c) & 15;
    const unsigned l0 = (unsigned)(uintptr_t)lds;
    unsigned long long ts0 = __builtin_amdgcn_s_memrealtime(), ts1 = 0;
    const unsigned xoff = (unsigned)((16 * w + (lane >> 2)) * 1024 + (((lane & 3) ^ ((lane >> 4) & 3)) * 16));
    const char* xb = x + (size_t)r * 128 * 1024;
    const int t1 = w >> 1, kh = w & 1;
    const char* wb1; unsigned pitch1;
    if (t1 < 3) { wb1 = wqkv + (t1 * 512 + 32 * c) * 2; pitch1 = 3072; } else { wb1 = w1 + (128 * ch) * 2; pitch1 = 3584; }
    const unsigned woff1 = (unsigned)((kh * 16 + (lane >> 2)) * pitch1 + (lane & 3) * 16);
    const char* wb2 = w1 + (128 * ch + 32 * (1 + t1)) * 2;
    const unsigned woff2 = (unsigned)((kh * 16 + (lane >> 2)) * 3584 + (lane & 3) * 16);
    auto issue = [&](int i, unsigned slot) {
        const int kc = (i + rot) & 15;
        glds16(xb + kc * 64, xoff, slot + w * 1024);
        glds16(wb1 + (size_t)kc * 32 * pitch1, woff1, slot + 8192 + w * 1024);
        if (w < 6) glds16(wb2 + (size_t)kc * 32 * 3584, woff2, slot + 16384 + w * 1024);
    };
#pragma unroll
    for (int i = 0; i < 6; ++i) issue(i, l0 + i * K1_STAGE);

    unsigned long long ts4 = __builtin_amdgcn_s_memrealtime();
    const int tt = w & 3, half = w >> 2, hi = lane >> 5;
    f32x16 acc0 = {}, acc1 = {}, acc2, acc3;
    {
        typedef unsigned u32x16 __attribute__((ext_vector_type(16)));
        const unsigned* bp = b1 + (128 * ch + 64 * half) / 2;
        u32x16 bA, bB;
        asm volatile("s_load_dwordx16 %0, %2, 0x0\n\ts_load_dwordx16 %1, %2, 0x40\n\ts_waitcnt lgkmcnt(0)" : "=&s"(bA), "=&s"(bB) : "s"(bp) : "memory");
#pragma unroll
        for (int g = 0; g < 4; ++g)
#pragma unroll
            for (int j = 0; j < 4; ++j) {
                const unsigned a0 = hi ? bA[4 * g + 2 + (j >> 1)] : bA[4 * g + (j >> 1)];
                const unsigned a1 = hi ? bB[4 * g + 2 + (j >> 1)] : bB[4 * g + (j >> 1)];
                acc2[4 * g + j] = __uint_as_float((j & 1) ? (a0 & 0xffff0000u) : (a0 << 16));
                acc3[4 * g + j] = __uint_as_float((j & 1) ? (a1 & 0xffff0000u) : (a1 << 16));
            }
    }
    unsigned long long ts5 = __builtin_amdgcn_s_memrealtime(), ts6 = 0;
    const int xrow = 32 * tt + (lane & 31), sw = (lane >> 2) & 3;
    const unsigned xr0 = xrow * 64 + ((hi ^ sw) * 16), xr1 = xr0 ^ 32;
    const unsigned wr = 8192 + (8 * hi + ((lane & 15) >> 2)) * 64 + ((lane >> 4) & 1) * 32 + (lane & 3) * 8;
    auto mainloop = [&](auto HALF) {
#pragma unroll
        for (int i = 0; i < 16; ++i) {
            const int rem = (15 - i) < 5 ? (15 - i) : 5;
            if (HALF.value == 0 || w < 6) asm volatile("s_waitcnt vmcnt(%0)" :: "i"(3 * rem) : "memory");
            else asm volatile("s_waitcnt vmcnt(%0)" :: "i"(2 * rem) : "memory");
            BARRIER();
            if (i == 0) ts1 = __builtin_amdgcn_s_memrealtime(); if (i == 8) ts6 = __builtin_amdgcn_s_memrealtime();
            if (i + 6 < 16) issue(i + 6, l0 + ((i + 6) % K1_NS) * K1_STAGE);
            lds_cptr sp = (lds_cptr)lds + (i % K1_NS) * K1_STAGE;
#pragma unroll
            for (int ks = 0; ks < 2; ++ks) {
                const bf16x8 xf = ldfrag(sp + (ks ? xr1 : xr0));
                lds_cptr wp = sp + wr + ks * 1024;
                if (HALF.value == 0) {
                    acc0 = MFMA(trfrag(wp + 0 * 2048, 256), xf, acc0);
                    acc1 = MFMA(trfrag(wp + 1 * 2048, 256), xf, acc1);
                    acc2 = MFMA(trfrag(wp + 3 * 2048, 256), xf, acc2);
                    acc3 = MFMA(trfrag(wp + 4 * 2048, 256), xf, acc3);
                } else {
                    acc0 = MFMA(xf, trfrag(wp + 2 * 2048, 256), acc0);
                    acc2 = MFMA(trfrag(wp + 5 * 2048, 256), xf, acc2);
                    acc3 = MFMA(trfrag(wp + 6 * 2048, 256), xf, acc3);
                }
            }
        }
    };
    if (half == 0) mainloop(std::integral_constant<int, 0>{}); else mainloop(std::integral_constant<int, 1>{});
    unsigned long long ts2 = __builtin_amdgcn_s_memrealtime();
    const int tile = r * 4 + tt;
    if (half == 0) {
#pragma unroll
        for (int i = 0; i < 16; ++i) acc0[i] *= QSCALE;
#pragma unroll
        for (int s = 0; s < 2; ++s) {
            *(u32x4*)(ws + WS_Q + ((size_t)((2 * c + s) * 64 + tile) * 64 + lane) * 16) = pack8(acc0, s);
            *(u32x4*)(ws + WS_K + ((size_t)((2 * c + s) * 64 + tile) * 64 + lane) * 16) = pack8(acc1, s);
        }
    } else {
        const int head = 2 * c + ((lane >> 4) & 1);
#pragma unroll
        for (int s = 0; s < 2; ++s)
            *(u32x4*)(ws + WS_V + (size_t)(head * 64 + tile) * 1024 + s * 512 + hi * 256 + (lane & 15) * 16) = pack8(acc0, s);
    }
    *(u32x4*)(ws + WS_O + ((size_t)(b * 8 + w) * 64 + lane) * 16) = (u32x4){~0u, ~0u, ~0u, ~0u};
    if (c < 14) {
#pragma unroll
        for (int i = 0; i < 16; ++i) { acc2[i] = mish(acc2[i]); acc3[i] = mish(acc3[i]); }
        const int fs = 8 * c + 4 * half;
#pragma unroll
        for (int s = 0; s < 2; ++s) {
            *(u32x4*)(ws + WS_H + ((size_t)(tile * 112 + fs + s) * 64 + lane) * 16) = pack8(acc2, s);
            *(u32x4*)(ws + WS_H + ((size_t)(tile * 112 + fs + 2 + s) * 64 + lane) * 16) = pack8(acc3, s);
        }
    } else if (c == 15) {
        const u32x4 gv = *(const u32x4*)(gw + lane * 16);
        float gf[8];
#pragma unroll
        for (int j = 0; j < 4; ++j) { gf[2 * j] = __uint_as_float(gv[j] << 16); gf[2 * j + 1] = __uint_as_float(gv[j] & 0xffff0000u); }
        float mine = 0.f;
#pragma unroll
        for (int rr = 0; rr < 16; ++rr) {
            const u32x4 xv = *(const u32x4*)(xb + (size_t)(16 * w + rr) * 1024 + lane * 16);
            float s = 0.f;
#pragma unroll
            for (int j = 0; j < 4; ++j) { s = __builtin_fmaf(__uint_as_float(xv[j] << 16), gf[2 * j], s); s = __builtin_fmaf(__uint_as_float(xv[j] & 0xffff0000u), gf[2 * j + 1], s); }
#pragma unroll
            for (int m = 32; m >= 1; m >>= 1) s += __shfl_xor(s, m);
            if (lane == rr) mine = s;
        }
        if (lane < 16) ((float*)(ws + WS_G))[r * 128 + 16 * w + lane] = __builtin_amdgcn_rcpf(1.f + __builtin_amdgcn_exp2f(-mine * LOG2E));
    }
    KEND(0);
}

constexpr int A_K = 0, A_V = 65536, A_ONES = 131072, K2_LDS = 131072 + 2048;
DI float halfmax(float m) { auto rr = __builtin_amdgcn_permlane32_swap(__float_as_uint(m), __float_as_uint(m), false, false); return __builtin_fmaxf(__uint_as_float(rr[0]), __uint_as_float(rr[1])); }
DI float max16(const f32x16& p) {
    float a = __builtin_fmaxf(__builtin_fmaxf(p[0], p[1]), p[2]), b = __builtin_fmaxf(__builtin_fmaxf(p[3], p[4]), p[5]);
    a = __builtin_fmaxf(__builtin_fmaxf(a, p[6]), p[7]); b = __builtin_fmaxf(__builtin_fmaxf(b, p[8]), p[9]);
    a = __builtin_fmaxf(__builtin_fmaxf(a, p[10]), p[11]); b = __builtin_fmaxf(__builtin_fmaxf(b, p[12]), p[13]);
    a = __builtin_fmaxf(__builtin_fmaxf(a, p[14]), p[15]); return __builtin_fmaxf(a, b); }

__global__ __launch_bounds__(512, 2) void k2_attn(char* __restrict__ ws) {
    extern __shared__ __attribute__((aligned(16))) char lds[];
    KBEGIN();
    const int tid = threadIdx.x, lane = tid & 63, w = __builtin_amdgcn_readfirstlane(tid >> 6), hi = lane >> 5;
    const int b = blockIdx.x, head = (b & 7) * 4 + ((b >> 3) & 3), r = b >> 5;
    const unsigned l0 = (unsigned)(uintptr_t)lds;
    const char* kb = ws + WS_K + (size_t)head * 65536; const char* vb = ws + WS_V + (size_t)head * 65536;
    const int tile = r * 8 + w;
    bf16x8 qf; float g;
    asm volatile("global_load_dwordx4 %0, %1, %2" : "=v"(qf) : "v"(lane * 16), "s"(ws + WS_Q + (size_t)(head * 64 + tile) * 1024) : "memory");
    asm volatile("global_load_dword %0, %1, %2" : "=v"(g) : "v"((lane & 31) * 4), "s"(ws + WS_G + tile * 128) : "memory");
#pragma unroll
    for (int j = 0; j < 8; ++j) { glds16(kb + (j * 8 + w) * 1024, lane * 16, l0 + A_K + (j * 8 + w) * 1024); glds16(vb + (j * 8 + w) * 1024, lane * 16, l0 + A_V + (j * 8 + w) * 1024); }
    if (tid < 128) *(u32x4*)(lds + A_ONES + tid * 16) = (u32x4){0x3f803f80u, 0x3f803f80u, 0x3f803f80u, 0x3f803f80u};
    asm volatile("s_waitcnt vmcnt(14) lgkmcnt(0)\n\ts_barrier" : "+v"(qf), "+v"(g) :: "memory");
    lds_cptr kp = (lds_cptr)lds + A_K + lane * 16;
    const bool vl = (lane & 16) == 0;
    unsigned va = (vl ? A_V : A_ONES) + hi * 256 + (lane & 15) * 16; const unsigned vstep = vl ? 2048 : 0;
    f32x16 zero = {};
    f32x16 s0 = MFMA(ldfrag(kp), qf, zero);
    const float m = halfmax(max16(s0));
    f32x16 negm;
#pragma unroll
    for (int i = 0; i < 16; ++i) negm[i] = -m;
    f32x16 o = {};
    for (int t = 0; t < 64; t += 2) {
        if (t == 8) { WAITV(0); BARRIER(); }
#pragma unroll
        for (int u = 0; u < 2; ++u) {
            f32x16 s = MFMA(ldfrag(kp + (t + u) * 1024), qf, negm);
            lds_cptr vp = (lds_cptr)lds + va + u * 1024;
            const bf16x8 v0 = ldfrag(vp), v1 = ldfrag(vp + 512);
#pragma unroll
            for (int i = 0; i < 16; ++i) s[i] = __builtin_amdgcn_exp2f(s[i]);
            o = MFMA(v0, __builtin_bit_cast(bf16x8, pack8(s, 0)), o);
            o = MFMA(v1, __builtin_bit_cast(bf16x8, pack8(s, 1)), o);
        }
        va += vstep;
    }
    float big = __builtin_fabsf(o[8]);
#pragma unroll
    for (int i = 0; i < 8; ++i) big = __builtin_fmaxf(big, __builtin_fabsf(o[i]) * 0x1p-60f);
    if (__builtin_expect(__any(!(big < 0x1p60f) || !(o[8] > 0x1p-20f)), 0)) {
        float mr = -INFINITY; o = zero; va = (vl ? A_V : A_ONES) + hi * 256 + (lane & 15) * 16;
        for (int t = 0; t < 64; ++t) {
            f32x16 s = MFMA(ldfrag(kp + t * 1024), qf, zero);
            const float mn = __builtin_fmaxf(mr, halfmax(max16(s)));
            const float al = __builtin_amdgcn_exp2f(mr - mn); mr = mn;
#pragma unroll
            for (int i = 0; i < 16; ++i) { o[i] *= al; s[i] = __builtin_amdgcn_exp2f(s[i] - mn); }
            lds_cptr vp = (lds_cptr)lds + va;
            o = MFMA(ldfrag(vp), __builtin_bit_cast(bf16x8, pack8(s, 0)), o);
            o = MFMA(ldfrag(vp + 512), __builtin_bit_cast(bf16x8, pack8(s, 1)), o);
            va += vstep >> 1;
        }
    }
    const float sc = g * __builtin_amdgcn_rcpf(o[8]);
#pragma unroll
    for (int i = 0; i < 8; ++i) o[i] *= sc;
    *(u32x4*)(ws + WS_O + ((size_t)(tile * 32 + head) * 64 + lane) * 16) = pack8(o, 0);
    KEND(1);
}

constexpr int K3_LDS = 65536;
__global__ __launch_bounds__(256, 2) void k3_mlp(const char* __restrict__ w2, const char* __restrict__ wo, char* __restrict__ ws, char* __restrict__ out) {
    extern __shared__ __attribute__((aligned(16))) char lds[];
    KBEGIN();
    const int tid = threadIdx.x, lane = tid & 63, m = __builtin_amdgcn_readfirstlane(tid >> 6), hi = lane >> 5;
    const int b = blockIdx.x, rg = (b & 7) * 4 + ((b >> 3) & 3), ct = b >> 5;
    const int tA = (rg >> 2) * 8 + (rg & 3), tB = tA + 4;
    const unsigned ring = (unsigned)(uintptr_t)lds + m * 8192;
    const unsigned woff = (lane >> 2) * 1024 + (lane & 3) * 16;
    const char* w2b = w2 + (size_t)(28 * m) * 16 * 1024 + ct * 128;
    const char* hA = ws + WS_H + ((size_t)(tA * 112 + 28 * m) * 64) * 16;
    const char* hB = ws + WS_H + ((size_t)(tB * 112 + 28 * m) * 64) * 16;
    const unsigned lo = lane * 16;
    u32x4 ha[4], hb[4];
#define LDH(dst, base, off) asm volatile("global_load_dwordx4 %0, %1, %2" : "=v"(dst) : "v"(lo), "s"((base) + (off)) : "memory")
#define ISSUE(ks, sl) do { glds16(w2b + (size_t)(ks) * 16384, woff, ring + (sl) * 2048); glds16(w2b + (size_t)(ks) * 16384 + 64, woff, ring + (sl) * 2048 + 1024); \
        LDH(ha[sl], hA, (ks) * 1024); LDH(hb[sl], hB, (ks) * 1024); } while (0)
    float gA, gB;
    asm volatile("global_load_dword %0, %1, %2" : "=v"(gA) : "v"((lane & 31) * 4), "s"(ws + WS_G + tA * 128) : "memory");
    asm volatile("global_load_dword %0, %1, %2" : "=v"(gB) : "v"((lane & 31) * 4), "s"(ws + WS_G + tB * 128) : "memory");
    ISSUE(0, 0); ISSUE(1, 1); ISSUE(2, 2); ISSUE(3, 3);
    f32x16 a00 = {}, a01 = {}, a10 = {}, a11 = {};
    lds_cptr wr = (lds_cptr)lds + m * 8192 + (4 * hi + ((lane & 15) >> 2)) * 64 + ((lane >> 4) & 1) * 32 + (lane & 3) * 8;
#define STEP(sl, WAIT) do { asm volatile("s_waitcnt vmcnt(" #WAIT ")" : "+v"(ha[sl]), "+v"(hb[sl]) :: "memory"); \
        const bf16x8 f0 = trfrag(wr + (sl) * 2048, 512), f1 = trfrag(wr + (sl) * 2048 + 1024, 512); \
        const bf16x8 xa = __builtin_bit_cast(bf16x8, ha[sl]), xb = __builtin_bit_cast(bf16x8, hb[sl]); \
        a00 = MFMA(f0, xa, a00); a01 = MFMA(f0, xb, a01); a10 = MFMA(f1, xa, a10); a11 = MFMA(f1, xb, a11); } while (0)
#define LGK0() asm volatile("s_waitcnt lgkmcnt(0)" ::: "memory")
    for (int k = 4; k < 28; k += 4) {
        STEP(0, 12); LGK0(); ISSUE(k + 0, 0);
        STEP(1, 12); LGK0(); ISSUE(k + 1, 1);
        STEP(2, 12); LGK0(); ISSUE(k + 2, 2);
        STEP(3, 12); LGK0(); ISSUE(k + 3, 3);
    }
    STEP(0, 12); STEP(1, 8); STEP(2, 4); STEP(3, 0);
    asm volatile("" : "+v"(gA), "+v"(gB));
#pragma unroll
    for (int i = 0; i < 16; ++i) { a00[i] *= 1.f - gA; a10[i] *= 1.f - gA; a01[i] *= 1.f - gB; a11[i] *= 1.f - gB; }
    const char* wob = wo + (size_t)(8 * m) * 16 * 1024 + ct * 128;
    const char* oA = ws + WS_O + ((size_t)(tA * 32 + 8 * m) * 64) * 16;
    const char* oB = ws + WS_O + ((size_t)(tB * 32 + 8 * m) * 64) * 16;
#define ISSUE2(ks, sl) do { glds16(wob + (size_t)(ks) * 16384, woff, ring + (sl) * 2048); glds16(wob + (size_t)(ks) * 16384 + 64, woff, ring + (sl) * 2048 + 1024); \
        LDH(ha[sl], oA, (ks) * 1024); LDH(hb[sl], oB, (ks) * 1024); } while (0)
    asm volatile("s_waitcnt lgkmcnt(0)" ::: "memory");
    ISSUE2(0, 0); ISSUE2(1, 1); ISSUE2(2, 2); ISSUE2(3, 3);
    STEP(0, 12); asm volatile("s_waitcnt lgkmcnt(0)" ::: "memory"); ISSUE2(4, 0);
    STEP(1, 12); asm volatile("s_waitcnt lgkmcnt(0)" ::: "memory"); ISSUE2(5, 1);
    STEP(2, 12); asm volatile("s_waitcnt lgkmcnt(0)" ::: "memory"); ISSUE2(6, 2);
    STEP(3, 12); asm volatile("s_waitcnt lgkmcnt(0)" ::: "memory"); ISSUE2(7, 3);
    STEP(0, 12); STEP(1, 8); STEP(2, 4); STEP(3, 0);
    BARRIER();
    typedef float f32x4 __attribute__((ext_vector_type(4)));
    auto put = [&](const f32x16& a, int tileIdx) {
        if (tileIdx != m) {
#pragma unroll
            for (int q = 0; q < 4; ++q) *(f32x4*)(lds + ((tileIdx * 4 + m) * 4 + q) * 1024 + lane * 16) = (f32x4){a[4 * q], a[4 * q + 1], a[4 * q + 2], a[4 * q + 3]};
        } };
    put(a00, 0); put(a01, 1); put(a10, 2); put(a11, 3);
    BARRIER();
    f32x16 acc = m == 0 ? a00 : m == 1 ? a01 : m == 2 ? a10 : a11;
#pragma unroll
    for (int o = 1; o < 4; ++o) {
        const int src = (m + o) & 3;
#pragma unroll
        for (int q = 0; q < 4; ++q) { const f32x4 v = *(const f32x4*)(lds + ((m * 4 + src) * 4 + q) * 1024 + lane * 16); acc[4 * q] += v[0]; acc[4 * q + 1] += v[1]; acc[4 * q + 2] += v[2]; acc[4 * q + 3] += v[3]; }
    }
    const int tok = ((m & 1) ? tB : tA) * 32 + (lane & 31);
    char* op = out + (size_t)tok * 1024 + ct * 128 + (m >> 1) * 64 + hi * 16;
#pragma unroll
    for (int p = 0; p < 2; ++p) {
        unsigned a0 = cvtpk(acc[8 * p], acc[8 * p + 1]), a1 = cvtpk(acc[8 * p + 2], acc[8 * p + 3]);
        unsigned b0 = cvtpk(acc[8 * p + 4], acc[8 * p + 5]), b1 = cvtpk(acc[8 * p + 6], acc[8 * p + 7]);
        auto r0 = __builtin_amdgcn_permlane32_swap(a0, b0, false, false);
        auto r1 = __builtin_amdgcn_permlane32_swap(a1, b1, false, false);
        *(u32x4*)(op + p * 32) = (u32x4){r0[0], r1[0], r0[1], r1[1]};
    }
    KEND(2);
}

__global__ void reporter(char* ws) {
    const unsigned long long t0 = RT();
    const unsigned long long* p = (const unsigned long long*)(ws + WS_ST2);
    __shared__ unsigned lo_s[3][4], hi_s[3][4];
    const unsigned long long base = p[0] - 100000ull;
    unsigned lo[3], hi[3];
    for (int k = 0; k < 3; ++k) { const int nw = k == 2 ? 4 : 8; unsigned l = ~0u, h = 0; for (int w = 0; w < nw; ++w) { const unsigned long long* q = p + ((k * 256 + threadIdx.x) * 8 + w) * 4; l = min(l, (unsigned)(q[0] - base)); h = max(h, (unsigned)(q[1] - base)); }
        for (int m = 32; m >= 1; m >>= 1) { l = min(l, (unsigned)__shfl_xor((int)l, m)); h = max(h, (unsigned)__shfl_xor((int)h, m)); }
        if ((threadIdx.x & 63) == 0) { lo_s[k][threadIdx.x >> 6] = l; hi_s[k][threadIdx.x >> 6] = h; } }
    __syncthreads();
    for (int k = 0; k < 3; ++k) { lo[k] = min(min(lo_s[k][0], lo_s[k][1]), min(lo_s[k][2], lo_s[k][3])); hi[k] = max(max(hi_s[k][0], hi_s[k][1]), max(hi_s[k][2], hi_s[k][3])); }
    unsigned long long X = 0;
    if (SEL == 1) X = hi[0] - lo[0];
    if (SEL == 2) X = 4 * (lo[1] - hi[0]);
    if (SEL == 3) X = hi[1] - lo[1];
    if (SEL == 4) X = 4 * (lo[2] - hi[1]);
    if (SEL == 5) X = hi[2] - lo[2];
    if (SEL == 6) { const unsigned long long* q = p + (1 * 256 * 8) * 4; X = q[2] * 100 / (q[1] - q[0]); }
    if (SEL == 7) X = 4 * ((unsigned)(t0 - base) - hi[2]);
    if (X > 20000) X = 20000;
    while (RT() < t0 + 1000 + X) __builtin_amdgcn_s_sleep(1);
}
extern "C" void kernel_launch(void* const* d_in, const int* in_sizes, int n_in, void* d_out, int out_size, void* d_ws, size_t ws_size, hipStream_t stream) {
    (void)hipFuncSetAttribute((const void*)k1_proj, hipFuncAttributeMaxDynamicSharedMemorySize, K1_LDS);
    (void)hipFuncSetAttribute((const void*)k2_attn, hipFuncAttributeMaxDynamicSharedMemorySize, K2_LDS);
    (void)hipFuncSetAttribute((const void*)k3_mlp, hipFuncAttributeMaxDynamicSharedMemorySize, K3_LDS);
    k1_proj<<<256, 512, K1_LDS, stream>>>((const char*)d_in[0], (const char*)d_in[1], (const char*)d_in[4], (const unsigned*)d_in[5], (const char*)d_in[7], (char*)d_ws);
    k2_attn<<<256, 512, K2_LDS, stream>>>((char*)d_ws);
    k3_mlp<<<256, 256, K3_LDS, stream>>>((const char*)d_in[6], (const char*)d_in[2], (char*)d_ws, (char*)d_out);
    reporter<<<1,256,0,stream>>>((char*)d_ws);
}
```

```cpp
#define SEL 0
#include <hip/hip_runtime.h>
#include <hip/hip_bf16.h>
#include <stdint.h>
#include <type_traits>

typedef unsigned short u16;
using bf16x8 = __attribute__((ext_vector_type(8))) short;
using s16x4  = __attribute__((ext_vector_type(4))) short;
using f32x16 = __attribute__((ext_vector_type(16))) float;
using u32x4  = __attribute__((ext_vector_type(4))) unsigned;
using u32x2  = __attribute__((ext_vector_type(2))) unsigned;
#define DI __device__ __forceinline__
#define LAS __attribute__((address_space(3)))
#define MFMA(a, b, c) __builtin_amdgcn_mfma_f32_32x32x16_bf16((a), (b), (c), 0, 0, 0)
typedef LAS const char* lds_cptr;

constexpr int SEQ = 2048, DM = 512, FF = 1792, NH = 32;
constexpr size_t WS_Q = 0, WS_K = 2u << 20, WS_V = 4u << 20, WS_O = 6u << 20, WS_H = 8u << 20, WS_G = 16u << 20;
constexpr float QSCALE = 0.25f * 1.4426950408889634f;
constexpr float LOG2E = 1.4426950408889634f;

typedef float f32x2 __attribute__((ext_vector_type(2)));
typedef __bf16 bf16x2 __attribute__((ext_vector_type(2)));
DI unsigned cvtpk(float lo, float hi) { return __builtin_bit_cast(unsigned, __builtin_convertvector((f32x2){lo, hi}, bf16x2)); }
constexpr size_t WS_ST2 = 48u << 20;
#define RT() __builtin_amdgcn_s_memrealtime()
#define KBEGIN() const unsigned long long kb_t = RT(), kb_c = __builtin_amdgcn_s_memtime()
#define KEND(k) do { asm volatile("s_waitcnt vmcnt(0)" ::: "memory"); if ((threadIdx.x & 63) == 0) { unsigned long long* p_ = (unsigned long long*)(ws + WS_ST2) + (((k) * 256 + blockIdx.x) * 8 + (threadIdx.x >> 6)) * 4; p_[0] = kb_t; p_[1] = RT(); p_[2] = __builtin_amdgcn_s_memtime() - kb_c; } } while (0)
#define STAMP(kern, i)
DI u32x4 pack8(const f32x16& a, int s) { return (u32x4){cvtpk(a[8*s], a[8*s+1]), cvtpk(a[8*s+2], a[8*s+3]), cvtpk(a[8*s+4], a[8*s+5]), cvtpk(a[8*s+6], a[8*s+7])}; }
DI void glds16(const void* sbase, unsigned voff, unsigned lds_dst) {
    unsigned sv; asm volatile("s_mov_b32 %0, m0\n\ts_mov_b32 m0, %3\n\ts_nop 0\n\tglobal_load_lds_dwordx4 %1, %2\n\ts_mov_b32 m0, %0" : "=&s"(sv) : "v"(voff), "s"(sbase), "s"(lds_dst) : "memory"); }
typedef short v4i16_t __attribute__((ext_vector_type(4)));
DI s16x4 trd(lds_cptr p) { return __builtin_bit_cast(s16x4, __builtin_amdgcn_ds_read_tr16_b64_v4i16((LAS v4i16_t*)p)); }
DI bf16x8 trfrag(lds_cptr p, int second) { s16x4 a = trd(p), b = trd(p + second); return __builtin_shufflevector(a, b, 0, 1, 2, 3, 4, 5, 6, 7); }
DI bf16x8 ldfrag(lds_cptr p) { return *(LAS const bf16x8*)p; }
#define WAITV(n) asm volatile("s_waitcnt vmcnt(" #n ")" ::: "memory")
#define BARRIER() asm volatile("s_waitcnt lgkmcnt(0)\n\ts_barrier" ::: "memory")
DI float mish(float h) {
    const float u = __builtin_amdgcn_exp2f(__builtin_fminf(h * LOG2E, 40.f));
    const float n = u * (u + 2.f);
    return h * n * __builtin_amdgcn_rcpf(n + 2.f);
}

constexpr int K1_STAGE = 22 * 1024, K1_NS = 7, K1_LDS = K1_STAGE * K1_NS;
__global__ __launch_bounds__(512, 2) void k1_proj(const char* __restrict__ x, const char* __restrict__ wqkv, const char* __restrict__ w1,
                                                  const unsigned* __restrict__ b1, const char* __restrict__ gw, char* __restrict__ ws) {
    extern __shared__ __attribute__((aligned(16))) char lds[];
    KBEGIN();
    const int tid = threadIdx.x, lane = tid & 63, w = __builtin_amdgcn_readfirstlane(tid >> 6);
    const int b = blockIdx.x, xcd = b & 7, bi = b >> 3;
    const int r = (xcd & 1) * 8 + (bi & 7), c = (xcd >> 1) * 4 + (bi >> 3);
    const int ch = c < 14 ? c : 13;
    const int rot = (2 * r + 5 * c) & 15;
    const unsigned l0 = (unsigned)(uintptr_t)lds;
    unsigned long long ts0 = __builtin_amdgcn_s_memrealtime(), ts1 = 0;
    const unsigned xoff = (unsigned)((16 * w + (lane >> 2)) * 1024 + (((lane & 3) ^ ((lane >> 4) & 3)) * 16));
    const char* xb = x + (size_t)r * 128 * 1024;
    const int t1 = w >> 1, kh = w & 1;
    const char* wb1; unsigned pitch1;
    if (t1 < 3) { wb1 = wqkv + (t1 * 512 + 32 * c) * 2; pitch1 = 3072; } else { wb1 = w1 + (128 * ch) * 2; pitch1 = 3584; }
    const unsigned woff1 = (unsigned)((kh * 16 + (lane >> 2)) * pitch1 + (lane & 3) * 16);
    const char* wb2 = w1 + (128 * ch + 32 * (1 + t1)) * 2;
    const unsigned woff2 = (unsigned)((kh * 16 + (lane >> 2)) * 3584 + (lane & 3) * 16);
    auto issue = [&](int i, unsigned slot) {
        const int kc = (i + rot) & 15;
        glds16(xb + kc * 64, xoff, slot + w * 1024);
        glds16(wb1 + (size_t)kc * 32 * pitch1, woff1, slot + 8192 + w * 1024);
        if (w < 6) glds16(wb2 + (size_t)kc * 32 * 3584, woff2, slot + 16384 + w * 1024);
    };
#pragma unroll
    for (int i = 0; i < 6; ++i) issue(i, l0 + i * K1_STAGE);

    unsigned long long ts4 = __builtin_amdgcn_s_memrealtime();
    const int tt = w & 3, half = w >> 2, hi = lane >> 5;
    f32x16 acc0 = {}, acc1 = {}, acc2, acc3;
    {
        typedef unsigned u32x16 __attribute__((ext_vector_type(16)));
        const unsigned* bp = b1 + (128 * ch + 64 * half) / 2;
        u32x16 bA, bB;
        asm volatile("s_load_dwordx16 %0, %2, 0x0\n\ts_load_dwordx16 %1, %2, 0x40\n\ts_waitcnt lgkmcnt(0)" : "=&s"(bA), "=&s"(bB) : "s"(bp) : "memory");
#pragma unroll
        for (int g = 0; g < 4; ++g)
#pragma unroll
            for (int j = 0; j < 4; ++j) {
                const unsigned a0 = hi ? bA[4 * g + 2 + (j >> 1)] : bA[4 * g + (j >> 1)];
                const unsigned a1 = hi ? bB[4 * g + 2 + (j >> 1)] : bB[4 * g + (j >> 1)];
                acc2[4 * g + j] = __uint_as_float((j & 1) ? (a0 & 0xffff0000u) : (a0 << 16));
                acc3[4 * g + j] = __uint_as_float((j & 1) ? (a1 & 0xffff0000u) : (a1 << 16));
            }
    }
    unsigned long long ts5 = __builtin_amdgcn_s_memrealtime(), ts6 = 0;
    const int xrow = 32 * tt + (lane & 31), sw = (lane >> 2) & 3;
    const unsigned xr0 = xrow * 64 + ((hi ^ sw) * 16), xr1 = xr0 ^ 32;
    const unsigned wr = 8192 + (8 * hi + ((lane & 15) >> 2)) * 64 + ((lane >> 4) & 1) * 32 + (lane & 3) * 8;
    auto mainloop = [&](auto HALF) {
#pragma unroll
        for (int i = 0; i < 16; ++i) {
            const int rem = (15 - i) < 5 ? (15 - i) : 5;
            if (HALF.value == 0 || w < 6) asm volatile("s_waitcnt vmcnt(%0)" :: "i"(3 * rem) : "memory");
            else asm volatile("s_waitcnt vmcnt(%0)" :: "i"(2 * rem) : "memory");
            BARRIER();
            if (i == 0) ts1 = __builtin_amdgcn_s_memrealtime(); if (i == 8) ts6 = __builtin_amdgcn_s_memrealtime();
            if (i + 6 < 16) issue(i + 6, l0 + ((i + 6) % K1_NS) * K1_STAGE);
            lds_cptr sp = (lds_cptr)lds + (i % K1_NS) * K1_STAGE;
#pragma unroll
            for (int ks = 0; ks < 2; ++ks) {
                const bf16x8 xf = ldfrag(sp + (ks ? xr1 : xr0));
                lds_cptr wp = sp + wr + ks * 1024;
                if (HALF.value == 0) {
                    acc0 = MFMA(trfrag(wp + 0 * 2048, 256), xf, acc0);
                    acc1 = MFMA(trfrag(wp + 1 * 2048, 256), xf, acc1);
                    acc2 = MFMA(trfrag(wp + 3 * 2048, 256), xf, acc2);
                    acc3 = MFMA(trfrag(wp + 4 * 2048, 256), xf, acc3);
                } else {
                    acc0 = MFMA(xf, trfrag(wp + 2 * 2048, 256), acc0);
                    acc2 = MFMA(trfrag(wp + 5 * 2048, 256), xf, acc2);
                    acc3 = MFMA(trfrag(wp + 6 * 2048, 256), xf, acc3);
                }
            }
        }
    };
    if (half == 0) mainloop(std::integral_constant<int, 0>{}); else mainloop(std::integral_constant<int, 1>{});
    unsigned long long ts2 = __builtin_amdgcn_s_memrealtime();
    const int tile = r * 4 + tt;
    if (half == 0) {
#pragma unroll
        for (int i = 0; i < 16; ++i) acc0[i] *= QSCALE;
#pragma unroll
        for (int s = 0; s < 2; ++s) {
            *(u32x4*)(ws + WS_Q + ((size_t)((2 * c + s) * 64 + tile) * 64 + lane) * 16) = pack8(acc0, s);
            *(u32x4*)(ws + WS_K + ((size_t)((2 * c + s) * 64 + tile) * 64 + lane) * 16) = pack8(acc1, s);
        }
    } else {
        const int head = 2 * c + ((lane >> 4) & 1);
#pragma unroll
        for (int s = 0; s < 2; ++s)
            *(u32x4*)(ws + WS_V + (size_t)(head * 64 + tile) * 1024 + s * 512 + hi * 256 + (lane & 15) * 16) = pack8(acc0, s);
    }
    *(u32x4*)(ws + WS_O + ((size_t)(b * 8 + w) * 64 + lane) * 16) = (u32x4){~0u, ~0u, ~0u, ~0u};
    if (c < 14) {
#pragma unroll
        for (int i = 0; i < 16; ++i) { acc2[i] = mish(acc2[i]); acc3[i] = mish(acc3[i]); }
        const int fs = 8 * c + 4 * half;
#pragma unroll
        for (int s = 0; s < 2; ++s) {
            *(u32x4*)(ws + WS_H + ((size_t)(tile * 112 + fs + s) * 64 + lane) * 16) = pack8(acc2, s);
            *(u32x4*)(ws + WS_H + ((size_t)(tile * 112 + fs + 2 + s) * 64 + lane) * 16) = pack8(acc3, s);
        }
    } else if (c == 15) {
        const u32x4 gv = *(const u32x4*)(gw + lane * 16);
        float gf[8];
#pragma unroll
        for (int j = 0; j < 4; ++j) { gf[2 * j] = __uint_as_float(gv[j] << 16); gf[2 * j + 1] = __uint_as_float(gv[j] & 0xffff0000u); }
        float mine = 0.f;
#pragma unroll
        for (int rr = 0; rr < 16; ++rr) {
            const u32x4 xv = *(const u32x4*)(xb + (size_t)(16 * w + rr) * 1024 + lane * 16);
            float s = 0.f;
#pragma unroll
            for (int j = 0; j < 4; ++j) { s = __builtin_fmaf(__uint_as_float(xv[j] << 16), gf[2 * j], s); s = __builtin_fmaf(__uint_as_float(xv[j] & 0xffff0000u), gf[2 * j + 1], s); }
#pragma unroll
            for (int m = 32; m >= 1; m >>= 1) s += __shfl_xor(s, m);
            if (lane == rr) mine = s;
        }
        if (lane < 16) ((float*)(ws + WS_G))[r * 128 + 16 * w + lane] = __builtin_amdgcn_rcpf(1.f + __builtin_amdgcn_exp2f(-mine * LOG2E));
    }
    KEND(0);
}

constexpr int A_K = 0, A_V = 65536, A_ONES = 131072, K2_LDS = 131072 + 2048;
DI float halfmax(float m) { auto rr = __builtin_amdgcn_permlane32_swap(__float_as_uint(m), __float_as_uint(m), false, false); return __builtin_fmaxf(__uint_as_float(rr[0]), __uint_as_float(rr[1])); }
DI float max16(const f32x16& p) {
    float a = __builtin_fmaxf(__builtin_fmaxf(p[0], p[1]), p[2]), b = __builtin_fmaxf(__builtin_fmaxf(p[3], p[4]), p[5]);
    a = __builtin_fmaxf(__builtin_fmaxf(a, p[6]), p[7]); b = __builtin_fmaxf(__builtin_fmaxf(b, p[8]), p[9]);
    a = __builtin_fmaxf(__builtin_fmaxf(a, p[10]), p[11]); b = __builtin_fmaxf(__builtin_fmaxf(b, p[12]), p[13]);
    a = __builtin_fmaxf(__builtin_fmaxf(a, p[14]), p[15]); return __builtin_fmaxf(a, b); }

__global__ __launch_bounds__(512, 2) void k2_attn(char* __restrict__ ws) {
    extern __shared__ __attribute__((aligned(16))) char lds[];
    KBEGIN();
    const int tid = threadIdx.x, lane = tid & 63, w = __builtin_amdgcn_readfirstlane(tid >> 6), hi = lane >> 5;
    const int b = blockIdx.x, head = (b & 7) * 4 + ((b >> 3) & 3), r = b >> 5;
    const unsigned l0 = (unsigned)(uintptr_t)lds;
    const char* kb = ws + WS_K + (size_t)head * 65536; const char* vb = ws + WS_V + (size_t)head * 65536;
    const int tile = r * 8 + w;
    bf16x8 qf; float g;
    asm volatile("global_load_dwordx4 %0, %1, %2" : "=v"(qf) : "v"(lane * 16), "s"(ws + WS_Q + (size_t)(head * 64 + tile) * 1024) : "memory");
    asm volatile("global_load_dword %0, %1, %2" : "=v"(g) : "v"((lane & 31) * 4), "s"(ws + WS_G + tile * 128) : "memory");
#pragma unroll
    for (int j = 0; j < 8; ++j) { glds16(kb + (j * 8 + w) * 1024, lane * 16, l0 + A_K + (j * 8 + w) * 1024); glds16(vb + (j * 8 + w) * 1024, lane * 16, l0 + A_V + (j * 8 + w) * 1024); }
    if (tid < 128) *(u32x4*)(lds + A_ONES + tid * 16) = (u32x4){0x3f803f80u, 0x3f803f80u, 0x3f803f80u, 0x3f803f80u};
    asm volatile("s_waitcnt vmcnt(14) lgkmcnt(0)\n\ts_barrier" : "+v"(qf), "+v"(g) :: "memory");
    lds_cptr kp = (lds_cptr)lds + A_K + lane * 16;
    const bool vl = (lane & 16) == 0;
    unsigned va = (vl ? A_V : A_ONES) + hi * 256 + (lane & 15) * 16; const unsigned vstep = vl ? 2048 : 0;
    f32x16 zero = {};
    f32x16 s0 = MFMA(ldfrag(kp), qf, zero);
    const float m = halfmax(max16(s0));
    f32x16 negm;
#pragma unroll
    for (int i = 0; i < 16; ++i) negm[i] = -m;
    f32x16 o = {};
    for (int t = 0; t < 64; t += 2) {
        if (t == 8) { WAITV(0); BARRIER(); }
#pragma unroll
        for (int u = 0; u < 2; ++u) {
            f32x16 s = MFMA(ldfrag(kp + (t + u) * 1024), qf, negm);
            lds_cptr vp = (lds_cptr)lds + va + u * 1024;
            const bf16x8 v0 = ldfrag(vp), v1 = ldfrag(vp + 512);
#pragma unroll
            for (int i = 0; i < 16; ++i) s[i] = __builtin_amdgcn_exp2f(s[i]);
            o = MFMA(v0, __builtin_bit_cast(bf16x8, pack8(s, 0)), o);
            o = MFMA(v1, __builtin_bit_cast(bf16x8, pack8(s, 1)), o);
        }
        va += vstep;
    }
    float big = __builtin_fabsf(o[8]);
#pragma unroll
    for (int i = 0; i < 8; ++i) big = __builtin_fmaxf(big, __builtin_fabsf(o[i]) * 0x1p-60f);
    if (__builtin_expect(__any(!(big < 0x1p60f) || !(o[8] > 0x1p-20f)), 0)) {
        float mr = -INFINITY; o = zero; va = (vl ? A_V : A_ONES) + hi * 256 + (lane & 15) * 16;
        for (int t = 0; t < 64; ++t) {
            f32x16 s = MFMA(ldfrag(kp + t * 1024), qf, zero);
            const float mn = __builtin_fmaxf(mr, halfmax(max16(s)));
            const float al = __builtin_amdgcn_exp2f(mr - mn); mr = mn;
#pragma unroll
            for (int i = 0; i < 16; ++i) { o[i] *= al; s[i] = __builtin_amdgcn_exp2f(s[i] - mn); }
            lds_cptr vp = (lds_cptr)lds + va;
            o = MFMA(ldfrag(vp), __builtin_bit_cast(bf16x8, pack8(s, 0)), o);
            o = MFMA(ldfrag(vp + 512), __builtin_bit_cast(bf16x8, pack8(s, 1)), o);
            va += vstep >> 1;
        }
    }
    const float sc = g * __builtin_amdgcn_rcpf(o[8]);
#pragma unroll
    for (int i = 0; i < 8; ++i) o[i] *= sc;
    *(u32x4*)(ws + WS_O + ((size_t)(tile * 32 + head) * 64 + lane) * 16) = pack8(o, 0);
    KEND(1);
}

constexpr int K3_LDS = 65536;
__global__ __launch_bounds__(256, 2) void k3_mlp(const char* __restrict__ w2, const char* __restrict__ wo, char* __restrict__ ws, char* __restrict__ out) {
    extern __shared__ __attribute__((aligned(16))) char lds[];
    KBEGIN();
    const int tid = threadIdx.x, lane = tid & 63, m = __builtin_amdgcn_readfirstlane(tid >> 6), hi = lane >> 5;
    const int b = blockIdx.x, rg = (b & 7) * 4 + ((b >> 3) & 3), ct = b >> 5;
    const int tA = (rg >> 2) * 8 + (rg & 3), tB = tA + 4;
    const unsigned ring = (unsigned)(uintptr_t)lds + m * 8192;
    const unsigned woff = (lane >> 2) * 1024 + (lane & 3) * 16;
    const char* w2b = w2 + (size_t)(28 * m) * 16 * 1024 + ct * 128;
    const char* hA = ws + WS_H + ((size_t)(tA * 112 + 28 * m) * 64) * 16;
    const char* hB = ws + WS_H + ((size_t)(tB * 112 + 28 * m) * 64) * 16;
    const unsigned lo = lane * 16;
    u32x4 ha[4], hb[4];
#define LDH(dst, base, off) asm volatile("global_load_dwordx4 %0, %1, %2" : "=v"(dst) : "v"(lo), "s"((base) + (off)) : "memory")
#define ISSUE(ks, sl) do { glds16(w2b + (size_t)(ks) * 16384, woff, ring + (sl) * 2048); glds16(w2b + (size_t)(ks) * 16384 + 64, woff, ring + (sl) * 2048 + 1024); \
        LDH(ha[sl], hA, (ks) * 1024); LDH(hb[sl], hB, (ks) * 1024); } while (0)
    float gA, gB;
    asm volatile("global_load_dword %0, %1, %2" : "=v"(gA) : "v"((lane & 31) * 4), "s"(ws + WS_G + tA * 128) : "memory");
    asm volatile("global_load_dword %0, %1, %2" : "=v"(gB) : "v"((lane & 31) * 4), "s"(ws + WS_G + tB * 128) : "memory");
    ISSUE(0, 0); ISSUE(1, 1); ISSUE(2, 2); ISSUE(3, 3);
    f32x16 a00 = {}, a01 = {}, a10 = {}, a11 = {};
    lds_cptr wr = (lds_cptr)lds + m * 8192 + (4 * hi + ((lane & 15) >> 2)) * 64 + ((lane >> 4) & 1) * 32 + (lane & 3) * 8;
#define STEP(sl, WAIT) do { asm volatile("s_waitcnt vmcnt(" #WAIT ")" : "+v"(ha[sl]), "+v"(hb[sl]) :: "memory"); \
        const bf16x8 f0 = trfrag(wr + (sl) * 2048, 512), f1 = trfrag(wr + (sl) * 2048 + 1024, 512); \
        const bf16x8 xa = __builtin_bit_cast(bf16x8, ha[sl]), xb = __builtin_bit_cast(bf16x8, hb[sl]); \
        a00 = MFMA(f0, xa, a00); a01 = MFMA(f0, xb, a01); a10 = MFMA(f1, xa, a10); a11 = MFMA(f1, xb, a11); } while (0)
#define LGK0() asm volatile("s_waitcnt lgkmcnt(0)" ::: "memory")
    for (int k = 4; k < 28; k += 4) {
        STEP(0, 12); LGK0(); ISSUE(k + 0, 0);
        STEP(1, 12); LGK0(); ISSUE(k + 1, 1);
        STEP(2, 12); LGK0(); ISSUE(k + 2, 2);
        STEP(3, 12); LGK0(); ISSUE(k + 3, 3);
    }
    STEP(0, 12); STEP(1, 8); STEP(2, 4); STEP(3, 0);
    asm volatile("" : "+v"(gA), "+v"(gB));
#pragma unroll
    for (int i = 0; i < 16; ++i) { a00[i] *= 1.f - gA; a10[i] *= 1.f - gA; a01[i] *= 1.f - gB; a11[i] *= 1.f - gB; }
    const char* wob = wo + (size_t)(8 * m) * 16 * 1024 + ct * 128;
    const char* oA = ws + WS_O + ((size_t)(tA * 32 + 8 * m) * 64) * 16;
    const char* oB = ws + WS_O + ((size_t)(tB * 32 + 8 * m) * 64) * 16;
#define ISSUE2(ks, sl) do { glds16(wob + (size_t)(ks) * 16384, woff, ring + (sl) * 2048); glds16(wob + (size_t)(ks) * 16384 + 64, woff, ring + (sl) * 2048 + 1024); \
        LDH(ha[sl], oA, (ks) * 1024); LDH(hb[sl], oB, (ks) * 1024); } while (0)
    asm volatile("s_waitcnt lgkmcnt(0)" ::: "memory");
    ISSUE2(0, 0); ISSUE2(1, 1); ISSUE2(2, 2); ISSUE2(3, 3);
    STEP(0, 12); asm volatile("s_waitcnt lgkmcnt(0)" ::: "memory"); ISSUE2(4, 0);
    STEP(1, 12); asm volatile("s_waitcnt lgkmcnt(0)" ::: "memory"); ISSUE2(5, 1);
    STEP(2, 12); asm volatile("s_waitcnt lgkmcnt(0)" ::: "memory"); ISSUE2(6, 2);
    STEP(3, 12); asm volatile("s_waitcnt lgkmcnt(0)" ::: "memory"); ISSUE2(7, 3);
    STEP(0, 12); STEP(1, 8); STEP(2, 4); STEP(3, 0);
    BARRIER();
    typedef float f32x4 __attribute__((ext_vector_type(4)));
    auto put = [&](const f32x16& a, int tileIdx) {
        if (tileIdx != m) {
#pragma unroll
            for (int q = 0; q < 4; ++q) *(f32x4*)(lds + ((tileIdx * 4 + m) * 4 + q) * 1024 + lane * 16) = (f32x4){a[4 * q], a[4 * q + 1], a[4 * q + 2], a[4 * q + 3]};
        } };
    put(a00, 0); put(a01, 1); put(a10, 2); put(a11, 3);
    BARRIER();
    f32x16 acc = m == 0 ? a00 : m == 1 ? a01 : m == 2 ? a10 : a11;
#pragma unroll
    for (int o = 1; o < 4; ++o) {
        const int src = (m + o) & 3;
#pragma unroll
        for (int q = 0; q < 4; ++q) { const f32x4 v = *(const f32x4*)(lds + ((m * 4 + src) * 4 + q) * 1024 + lane * 16); acc[4 * q] += v[0]; acc[4 * q + 1] += v[1]; acc[4 * q + 2] += v[2]; acc[4 * q + 3] += v[3]; }
    }
    const int tok = ((m & 1) ? tB : tA) * 32 + (lane & 31);
    char* op = out + (size_t)tok * 1024 + ct * 128 + (m >> 1) * 64 + hi * 16;
#pragma unroll
    for (int p = 0; p < 2; ++p) {
        unsigned a0 = cvtpk(acc[8 * p], acc[8 * p + 1]), a1 = cvtpk(acc[8 * p + 2], acc[8 * p + 3]);
        unsigned b0 = cvtpk(acc[8 * p + 4], acc[8 * p + 5]), b1 = cvtpk(acc[8 * p + 6], acc[8 * p + 7]);
        auto r0 = __builtin_amdgcn_permlane32_swap(a0, b0, false, false);
        auto r1 = __builtin_amdgcn_permlane32_swap(a1, b1, false, false);
        *(u32x4*)(op + p * 32) = (u32x4){r0[0], r1[0], r0[1], r1[1]};
    }
    KEND(2);
}

__global__ void reporter(char* ws) {
    const unsigned long long t0 = RT();
    const unsigned long long* p = (const unsigned long long*)(ws + WS_ST2);
    __shared__ unsigned lo_s[3][4], hi_s[3][4];
    const unsigned long long base = p[0] - 100000ull;
    unsigned lo[3], hi[3];
    for (int k = 0; k < 3; ++k) { const int nw = k == 2 ? 4 : 8; unsigned l = ~0u, h = 0; for (int w = 0; w < nw; ++w) { const unsigned long long* q = p + ((k * 256 + threadIdx.x) * 8 + w) * 4; l = min(l, (unsigned)(q[0] - base)); h = max(h, (unsigned)(q[1] - base)); }
        for (int m = 32; m >= 1; m >>= 1) { l = min(l, (unsigned)__shfl_xor((int)l, m)); h = max(h, (unsigned)__shfl_xor((int)h, m)); }
        if ((threadIdx.x & 63) == 0) { lo_s[k][threadIdx.x >> 6] = l; hi_s[k][threadIdx.x >> 6] = h; } }
    __syncthreads();
    for (int k = 0; k < 3; ++k) { lo[k] = min(min(lo_s[k][0], lo_s[k][1]), min(lo_s[k][2], lo_s[k][3])); hi[k] = max(max(hi_s[k][0], hi_s[k][1]), max(hi_s[k][2], hi_s[k][3])); }
    __shared__ unsigned ex[4][4];
    { const int k = (SEL == 11) ? 0 : 1; const unsigned long long* q = p + ((k * 256 + threadIdx.x) * 8) * 4; unsigned st = 0, du = 0; for (int w = 0; w < 8; ++w) { st = max(st, (unsigned)(q[w * 4] - base)); du = max(du, (unsigned)(q[w * 4 + 1] - q[w * 4])); }
      unsigned ck = (unsigned)(q[2] * 100 / (q[1] - q[0])), ck2 = ck;
      for (int m = 32; m >= 1; m >>= 1) { st = max(st, (unsigned)__shfl_xor((int)st, m)); du = max(du, (unsigned)__shfl_xor((int)du, m)); ck = min(ck, (unsigned)__shfl_xor((int)ck, m)); ck2 = max(ck2, (unsigned)__shfl_xor((int)ck2, m)); }
      if ((threadIdx.x & 63) == 0) { ex[0][threadIdx.x >> 6] = st; ex[1][threadIdx.x >> 6] = du; ex[2][threadIdx.x >> 6] = ck; ex[3][threadIdx.x >> 6] = ck2; } }
    __syncthreads();
    unsigned long long X = 0;
    if (SEL == 8) X = 4 * (max(max(ex[0][0], ex[0][1]), max(ex[0][2], ex[0][3])) - lo[1]);
    if (SEL == 11) X = 4 * (max(max(ex[0][0], ex[0][1]), max(ex[0][2], ex[0][3])) - lo[0]);
    if (SEL == 9) X = min(min(ex[2][0], ex[2][1]), min(ex[2][2], ex[2][3]));
    if (SEL == 12) X = max(max(ex[3][0], ex[3][1]), max(ex[3][2], ex[3][3]));
    if (SEL == 10) X = max(max(ex[1][0], ex[1][1]), max(ex[1][2], ex[1][3]));
    if (SEL == 1) X = hi[0] - lo[0];
    if (SEL == 2) X = 4 * (lo[1] - hi[0]);
    if (SEL == 3) X = hi[1] - lo[1];
    if (SEL == 4) X = 4 * (lo[2] - hi[1]);
    if (SEL == 5) X = hi[2] - lo[2];
    if (SEL == 6) { const unsigned long long* q = p + (1 * 256 * 8) * 4; X = q[2] * 100 / (q[1] - q[0]); }
    if (SEL == 7) X = 4 * ((unsigned)(t0 - base) - hi[2]);
    if (X > 20000) X = 20000;
    while (RT() < t0 + 1000 + X) __builtin_amdgcn_s_sleep(1);
}
extern "C" void kernel_launch(void* const* d_in, const int* in_sizes, int n_in, void* d_out, int out_size, void* d_ws, size_t ws_size, hipStream_t stream) {
    (void)hipFuncSetAttribute((const void*)k1_proj, hipFuncAttributeMaxDynamicSharedMemorySize, K1_LDS);
    (void)hipFuncSetAttribute((const void*)k2_attn, hipFuncAttributeMaxDynamicSharedMemorySize, K2_LDS);
    (void)hipFuncSetAttribute((const void*)k3_mlp, hipFuncAttributeMaxDynamicSharedMemorySize, K3_LDS);
    k1_proj<<<256, 512, K1_LDS, stream>>>((const char*)d_in[0], (const char*)d_in[1], (const char*)d_in[4], (const unsigned*)d_in[5], (const char*)d_in[7], (char*)d_ws);
    k2_attn<<<256, 512, K2_LDS, stream>>>((char*)d_ws);
    k3_mlp<<<256, 256, K3_LDS, stream>>>((const char*)d_in[6], (const char*)d_in[2], (char*)d_ws, (char*)d_out);
    reporter<<<1,256,0,stream>>>((char*)d_ws);
}
```

```cpp
#include <hip/hip_runtime.h>
#include <hip/hip_bf16.h>
#include <stdint.h>
#include <type_traits>

using bf16x8 = __attribute__((ext_vector_type(8))) short;
using s16x4  = __attribute__((ext_vector_type(4))) short;
using f32x16 = __attribute__((ext_vector_type(16))) float;
using f32x4  = __attribute__((ext_vector_type(4))) float;
using f32x2  = __attribute__((ext_vector_type(2))) float;
using u32x4  = __attribute__((ext_vector_type(4))) unsigned;
using u32x2  = __attribute__((ext_vector_type(2))) unsigned;
typedef __bf16 bf16x2 __attribute__((ext_vector_type(2)));
typedef short v4i16_t __attribute__((ext_vector_type(4)));
typedef unsigned long long u64;
#define DI __device__ __forceinline__
#define LAS __attribute__((address_space(3)))
#define GAS __attribute__((address_space(1)))
#define MFMA(a, b, c) __builtin_amdgcn_mfma_f32_32x32x16_bf16((a), (b), (c), 0, 0, 0)
typedef LAS char* lds_ptr;

constexpr unsigned WS_Q = 0, WS_K = 2u << 20, WS_V = 4u << 20, WS_O = 6u << 20, WS_H = 8u << 20, WS_F = 16u << 20;
constexpr unsigned WS_BYTES = (16u << 20) + 65536;
constexpr unsigned F_QKV = WS_F, F_H = WS_F + 4096, F_O = WS_F + 16384, F_EPOCH = WS_F + 40960;
constexpr unsigned L_K = 0, L_V = 65536, L_B = 131072, L_MISC = L_B + 6144, LDS_BYTES = 163840;
#define PRIO_DROP 11
constexpr unsigned SPIN_CAP = 1u << 16;
constexpr float LOG2E = 1.4426950408889634f;
constexpr float QSCALE = 0.25f * LOG2E;

DI unsigned cvtpk(float lo, float hi) { return __builtin_bit_cast(unsigned, __builtin_convertvector((f32x2){lo, hi}, bf16x2)); }
DI u32x4 pack8(const f32x16& a, int s) { return (u32x4){cvtpk(a[8*s], a[8*s+1]), cvtpk(a[8*s+2], a[8*s+3]), cvtpk(a[8*s+4], a[8*s+5]), cvtpk(a[8*s+6], a[8*s+7])}; }
DI void glds16(const void* sbase, unsigned voff, unsigned lds_dst) {
    unsigned sv; asm volatile("s_mov_b32 %0, m0\n\ts_mov_b32 m0, %3\n\ts_nop 0\n\tglobal_load_lds_dwordx4 %1, %2\n\ts_mov_b32 m0, %0" : "=&s"(sv) : "v"(voff), "s"(sbase), "s"(lds_dst) : "memory"); }
DI void glds16_dev(const void* sbase, unsigned voff, unsigned lds_dst) {
    unsigned sv; asm volatile("s_mov_b32 %0, m0\n\ts_mov_b32 m0, %3\n\ts_nop 0\n\tglobal_load_lds_dwordx4 %1, %2 sc1\n\ts_mov_b32 m0, %0" : "=&s"(sv) : "v"(voff), "s"(sbase), "s"(lds_dst) : "memory"); }
DI void GLD(u32x4& dst, const void* sbase, unsigned voff) { asm volatile("global_load_dwordx4 %0, %1, %2" : "=v"(dst) : "v"(voff), "s"(sbase) : "memory"); }
DI void GLD_DEV(u32x4& dst, const void* sbase, unsigned voff) { asm volatile("global_load_dwordx4 %0, %1, %2 sc1" : "=v"(dst) : "v"(voff), "s"(sbase) : "memory"); }
DI void gst_dev(const void* sbase, unsigned voff, u32x4 v) { asm volatile("global_store_dwordx4 %0, %1, %2 sc1\n\ts_nop 1" :: "v"(voff), "v"(v), "s"(sbase) : "memory"); }
DI s16x4 trd(lds_ptr p) { return __builtin_bit_cast(s16x4, __builtin_amdgcn_ds_read_tr16_b64_v4i16((LAS v4i16_t*)p)); }
DI bf16x8 trfrag(lds_ptr p, int second) { s16x4 a = trd(p), b = trd(p + second); return __builtin_shufflevector(a, b, 0, 1, 2, 3, 4, 5, 6, 7); }
DI bf16x8 ldfrag(lds_ptr p) { return *(LAS const bf16x8*)p; }
DI bf16x8 asfrag(u32x4 v) { return __builtin_bit_cast(bf16x8, v); }
#define PACE_N 1
#define PACE() do { __builtin_amdgcn_sched_barrier(0); if (PACE_N) __builtin_amdgcn_s_sleep(PACE_N); __builtin_amdgcn_sched_barrier(0); } while (0)
#define WAITV(n) asm volatile("s_waitcnt vmcnt(%0)" :: "i"(n) : "memory")
#define BARRIER() do { asm volatile("" ::: "memory"); __builtin_amdgcn_s_waitcnt(0xC07F); __builtin_amdgcn_s_barrier(); asm volatile("" ::: "memory"); } while (0)
DI float mish(float h) {
    const float u = __builtin_amdgcn_exp2f(__builtin_fminf(h * LOG2E, 40.f));
    const float n = u * (u + 2.f);
    return h * n * __builtin_amdgcn_rcpf(n + 2.f);
}
DI float halfmax(float m) { auto rr = __builtin_amdgcn_permlane32_swap(__float_as_uint(m), __float_as_uint(m), false, false); return __builtin_fmaxf(__uint_as_float(rr[0]), __uint_as_float(rr[1])); }
DI float max16(const f32x16& p) {
    float a = __builtin_fmaxf(__builtin_fmaxf(p[0], p[1]), p[2]), b = __builtin_fmaxf(__builtin_fmaxf(p[3], p[4]), p[5]);
    a = __builtin_fmaxf(__builtin_fmaxf(a, p[6]), p[7]); b = __builtin_fmaxf(__builtin_fmaxf(b, p[8]), p[9]);
    a = __builtin_fmaxf(__builtin_fmaxf(a, p[10]), p[11]); b = __builtin_fmaxf(__builtin_fmaxf(b, p[12]), p[13]);
    a = __builtin_fmaxf(__builtin_fmaxf(a, p[14]), p[15]); return __builtin_fmaxf(a, b); }
DI void publish(char* ws, unsigned off, u64 tag, int lane) {
    asm volatile("s_waitcnt vmcnt(0)" ::: "memory");
    if (lane == 0) __hip_atomic_store((GAS u64*)(ws + off), tag, __ATOMIC_RELAXED, __HIP_MEMORY_SCOPE_AGENT);
}
DI void poll_tags(char* ws, unsigned off, int n, u64 tag, int lane) {
    GAS u64* p = (GAS u64*)(ws + off) + (lane < n ? lane : 0);
#pragma unroll 1
    for (unsigned s = 0; s < SPIN_CAP; ++s) {
        const u64 v = __hip_atomic_load(p, __ATOMIC_RELAXED, __HIP_MEMORY_SCOPE_AGENT);
        if (__all(v == tag)) break;
        __builtin_amdgcn_s_sleep(2);
    }
    asm volatile("" ::: "memory");
}
DI void lds_arrive(lds_ptr c, int lane) {
    asm volatile("s_waitcnt lgkmcnt(0)" ::: "memory");
    if (lane == 0) __hip_atomic_fetch_add((LAS unsigned*)c, 1u, __ATOMIC_RELAXED, __HIP_MEMORY_SCOPE_WORKGROUP);
}
DI void lds_wait(lds_ptr c, unsigned n) {
#pragma unroll 1
    for (unsigned s = 0; s < SPIN_CAP * 8; ++s) {
        const unsigned v = __builtin_amdgcn_readfirstlane(__hip_atomic_load((LAS unsigned*)c, __ATOMIC_RELAXED, __HIP_MEMORY_SCOPE_WORKGROUP));
        if (v >= n) break;
        __builtin_amdgcn_s_sleep(1);
    }
    asm volatile("" ::: "memory");
}
#define STAMP(i)
#define CSTAMP(i)
#define STAMP_DECL()
#define STAMP_FLUSH()
DI unsigned ximg(int kc) { return 32768u + (kc & 3) * 8192u + (kc >> 2) * 65536u; }
constexpr unsigned wimg(int n) { return n < 4 ? L_B + n * 8192u : n < 8 ? L_K + (n - 4) * 8192u : L_V + (n - 8) * 8192u; }

__global__ __launch_bounds__(768, 3) void fused_kernel(const char* __restrict__ x, const char* __restrict__ wqkv, const char* __restrict__ wo,
                                                       const char* __restrict__ w1, const unsigned* __restrict__ b1, const char* __restrict__ w2,
                                                       const char* __restrict__ gw, char* __restrict__ ws, char* __restrict__ out) {
    extern __shared__ __attribute__((aligned(16))) char lds_raw[];
    lds_ptr lds = (lds_ptr)lds_raw;
    const unsigned l0 = (unsigned)(uintptr_t)lds_raw;
    const int tid = threadIdx.x, lane = tid & 63, w = __builtin_amdgcn_readfirstlane(tid >> 6), hi = lane >> 5;
    const int b = blockIdx.x, xcd = b & 7, bi = b >> 3;
    const int tg = (xcd >> 1) * 8 + (bi & 7), hg = (xcd & 1) * 4 + (bi >> 3);
    const int rot = ((bi & 7) + 2 * (bi >> 3)) & 7;
    const unsigned lo16 = lane * 16;
    const unsigned xfo = (lane & 31) * 128 + ((hi ^ ((lane >> 1) & 7)) * 16);
    STAMP_DECL();
    STAMP(0);
    if (w < 8) {
        const int ct = (w < 4) ? (w & 1) : ((w >> 1) & 1);
        const unsigned wfo = (8 * hi + ((lane & 15) >> 2)) * 128 + ((ct ^ ((lane >> 3) & 1)) * 64) + ((lane >> 4) & 1) * 32 + (lane & 3) * 8;
        f32x16 acc0 = {}, acc1 = {};
        bf16x8 WF[2][4], XA[2][4], XC[2][4];
        if (w < 4) {
            const unsigned sgo = (w >> 1) * 8192u;
#pragma unroll
            for (int i = 0; i <= 8; ++i) {
                unsigned xb = 0, wb = 0;
                if (i < 8) {
                    BARRIER();
                    if (i == 0 || i == 7) STAMP(8 + i);
                    xb = ximg((i + rot) & 7) + xfo;
                    wb = ((i & 3) == 1 ? (w < 2 ? wimg(3) : wimg(4)) : wimg((i & 3) * 3) + sgo) + wfo;
                }
#pragma unroll
                for (int ks = 0; ks < 4; ++ks) {
                    if (i > 0) acc0 = MFMA(WF[(i - 1) & 1][ks], XA[(i - 1) & 1][ks], acc0);
                    if (i < 8) { WF[i & 1][ks] = trfrag(lds + wb + ks * 2048, 512); XA[i & 1][ks] = ldfrag(lds + (xb ^ (ks * 32))); }
                    __builtin_amdgcn_sched_barrier(0);
                    if (i > 0) acc1 = MFMA(WF[(i - 1) & 1][ks], XC[(i - 1) & 1][ks], acc1);
                    if (i < 8) XC[i & 1][ks] = ldfrag(lds + (xb ^ (ks * 32)) + 4096);
                    __builtin_amdgcn_sched_barrier(0);
                }
            }
            if (w < 2) {
#pragma unroll
                for (int i = 0; i < 16; ++i) { acc0[i] *= QSCALE; acc1[i] *= QSCALE; }
            }
            const char* dst = ws + (w < 2 ? WS_Q : WS_K) + (size_t)((4 * hg + 2 * ct) * 64 + 2 * tg) * 1024;
            gst_dev(dst, lo16, pack8(acc0, 0)); gst_dev(dst + 65536, lo16, pack8(acc0, 1));
            gst_dev(dst + 1024, lo16, pack8(acc1, 0)); gst_dev(dst + 65536 + 1024, lo16, pack8(acc1, 1));
        } else {
            const int tt = w & 1;
#pragma unroll
            for (int i = 0; i <= 8; ++i) {
                unsigned xb = 0;
                if (i < 8) {
                    BARRIER();
                    if (i == 0 || i == 7) STAMP(8 + i);
                    xb = ximg((i + rot) & 7) + xfo + tt * 4096;
                }
#pragma unroll
                for (int ks = 0; ks < 4; ++ks) {
                    if (i > 0) acc0 = MFMA(XA[(i - 1) & 1][ks], WF[(i - 1) & 1][ks], acc0);
                    if (i < 8) { WF[i & 1][ks] = trfrag(lds + wimg((i & 3) * 3 + 2) + wfo + ks * 2048, 512); XA[i & 1][ks] = ldfrag(lds + (xb ^ (ks * 32))); }
                    __builtin_amdgcn_sched_barrier(0);
                }
            }
            const char* dst = ws + WS_V + (size_t)((4 * hg + 2 * ct) * 64 + 2 * tg + tt) * 1024;
            const unsigned vo = ((lane >> 4) & 1) * 65536 + hi * 256 + (lane & 15) * 16;
            gst_dev(dst, vo, pack8(acc0, 0)); gst_dev(dst + 512, vo, pack8(acc0, 1));
        }
        STAMP(1);
        WAITV(0); BARRIER();
        STAMP(6);
        const u64 tag = *(LAS const u64*)(lds + L_MISC + 16);
        const int head = xcd * 4 + (bi & 3), r8 = bi >> 2;
        const int tile = 8 * r8 + (w < 4 ? 2 * w : 2 * (w - 4) + 1);
        if (w == 0) {
            if (lane == 0) __hip_atomic_store((GAS u64*)(ws + F_QKV + (hg * 32 + tg) * 8), tag, __ATOMIC_RELAXED, __HIP_MEMORY_SCOPE_AGENT);
            poll_tags(ws, F_QKV + (head >> 2) * 256, 32, tag, lane);
            if (lane == 0) __hip_atomic_store((LAS unsigned*)(lds + L_MISC + 24), 1u, __ATOMIC_RELAXED, __HIP_MEMORY_SCOPE_WORKGROUP);
        } else lds_wait(lds + L_MISC + 24, 1);
        STAMP(2);
        u32x4 qr;
        GLD_DEV(qr, ws + WS_Q + (size_t)(head * 64 + tile) * 1024, lo16);
        const char* kb = ws + WS_K + (size_t)head * 65536; const char* vb = ws + WS_V + (size_t)head * 65536;
#pragma unroll
        for (int j = 0; j < 4; ++j) { glds16_dev(kb + (j * 8 + w) * 1024, lo16, l0 + L_K + (j * 8 + w) * 1024); glds16_dev(vb + (j * 8 + w) * 1024, lo16, l0 + L_V + (j * 8 + w) * 1024); }
        asm volatile("s_waitcnt vmcnt(6)" : "+v"(qr) :: "memory");
        const bf16x8 qf = asfrag(qr);
        lds_arrive(lds + L_MISC, lane); lds_wait(lds + L_MISC, 8);
        STAMP(3); CSTAMP(10);
        if (w >= 4) __builtin_amdgcn_s_setprio(1);
        const f32x16 zero = {};
        const float nm = -halfmax(max16(MFMA(ldfrag(lds + L_K + lo16), qf, zero)));
        float o[9];
        {
            unsigned ka = l0 + L_K + lo16, va = l0 + L_V + hi * 256 + (lane & 15) * 16; const unsigned cnt_addr = l0 + L_MISC;
            unsigned s_it, s_sp, s_st;
            const unsigned onev = ((lane & 27) == 16) ? 0x3f803f80u : 0u;
    asm volatile(
        "ds_read_b128 v[136:139], %[ka]\n\t"
        "ds_read_b128 v[140:143], %[ka] offset:1024\n\t"
        "v_mov_b32 v144, %[one]\n\t"
        "v_mov_b32 v148, %[one]\n\t"
        "v_mov_b32 v145, %[one]\n\t"
        "v_mov_b32 v149, %[one]\n\t"
        "v_mov_b32 v146, %[one]\n\t"
        "v_mov_b32 v150, %[one]\n\t"
        "v_mov_b32 v147, %[one]\n\t"
        "v_mov_b32 v151, %[one]\n\t"
        "v_mov_b32 v152, %[one]\n\t"
        "v_mov_b32 v156, %[one]\n\t"
        "v_mov_b32 v153, %[one]\n\t"
        "v_mov_b32 v157, %[one]\n\t"
        "v_mov_b32 v154, %[one]\n\t"
        "v_mov_b32 v158, %[one]\n\t"
        "v_mov_b32 v155, %[one]\n\t"
        "v_mov_b32 v159, %[one]\n\t"
        "s_mov_b64 exec, %[vm]\n\t"
        "ds_read_b128 v[144:147], %[va] offset:0\n\t"
        "ds_read_b128 v[148:151], %[va] offset:512\n\t"
        "s_mov_b64 exec, -1\n\t"
        "v_mov_b32 v112, %[nm]\n\t"
        "v_mov_b32 v113, %[nm]\n\t"
        "v_mov_b32 v114, %[nm]\n\t"
        "v_mov_b32 v115, %[nm]\n\t"
        "v_mov_b32 v116, %[nm]\n\t"
        "v_mov_b32 v117, %[nm]\n\t"
        "v_mov_b32 v118, %[nm]\n\t"
        "v_mov_b32 v119, %[nm]\n\t"
        "v_mov_b32 v120, %[nm]\n\t"
        "v_mov_b32 v121, %[nm]\n\t"
        "v_mov_b32 v122, %[nm]\n\t"
        "v_mov_b32 v123, %[nm]\n\t"
        "v_mov_b32 v124, %[nm]\n\t"
        "v_mov_b32 v125, %[nm]\n\t"
        "v_mov_b32 v126, %[nm]\n\t"
        "v_mov_b32 v127, %[nm]\n\t"
        "v_mov_b32 v96, 0\n\t"
        "v_mov_b32 v97, 0\n\t"
        "v_mov_b32 v98, 0\n\t"
        "v_mov_b32 v99, 0\n\t"
        "v_mov_b32 v100, 0\n\t"
        "v_mov_b32 v101, 0\n\t"
        "v_mov_b32 v102, 0\n\t"
        "v_mov_b32 v103, 0\n\t"
        "v_mov_b32 v104, 0\n\t"
        "v_mov_b32 v105, 0\n\t"
        "v_mov_b32 v106, 0\n\t"
        "v_mov_b32 v107, 0\n\t"
        "v_mov_b32 v108, 0\n\t"
        "v_mov_b32 v109, 0\n\t"
        "v_mov_b32 v110, 0\n\t"
        "v_mov_b32 v111, 0\n\t"
        "v_mov_b32 v132, 0\n\t"
        "v_mov_b32 v133, 0\n\t"
        "v_mov_b32 v134, 0\n\t"
        "v_mov_b32 v135, 0\n\t"
        "s_mov_b32 %[it], 0\n\t"
        "s_waitcnt lgkmcnt(3)\n\t"
        "v_mfma_f32_32x32x16_bf16 v[64:79], v[136:139], %[q], v[112:127]\n\t"
        "1:\n\t"
        "s_cmp_eq_u32 %[it], 1\n\t"
        "s_cbranch_scc1 3f\n\t"
        "s_cmp_eq_u32 %[it], 7\n\t"
        "s_cbranch_scc1 4f\n\t"
        "s_cmp_lg_u32 %[it], %[pd]\n\t"
        "s_cbranch_scc1 2f\n\t"
        "s_setprio 0\n\t"
        "2:\n\t"
        "s_waitcnt lgkmcnt(2)\n\t"
        "v_mfma_f32_32x32x16_bf16 v[80:95], v[140:143], %[q], v[112:127]\n\t"
        "ds_read_b128 v[136:139], %[ka] offset:2048\n\t"
        "v_exp_f32 v64, v64\n\t"
        "v_exp_f32 v65, v65\n\t"
        "v_exp_f32 v66, v66\n\t"
        "v_exp_f32 v67, v67\n\t"
        "v_exp_f32 v68, v68\n\t"
        "v_exp_f32 v69, v69\n\t"
        "v_mfma_f32_32x32x16_bf16 v[96:111], v[156:159], v[132:135], v[96:111]\n\t"
        "s_mov_b64 exec, %[vm]\n\t"
        "ds_read_b128 v[152:155], %[va] offset:1024\n\t"
        "ds_read_b128 v[156:159], %[va] offset:1536\n\t"
        "s_mov_b64 exec, -1\n\t"
        "v_exp_f32 v70, v70\n\t"
        "v_exp_f32 v71, v71\n\t"
        "v_cvt_pk_bf16_f32 v128, v64, v65\n\t"
        "v_cvt_pk_bf16_f32 v129, v66, v67\n\t"
        "v_cvt_pk_bf16_f32 v130, v68, v69\n\t"
        "v_cvt_pk_bf16_f32 v131, v70, v71\n\t"
        "v_exp_f32 v72, v72\n\t"
        "v_exp_f32 v73, v73\n\t"
        "v_exp_f32 v74, v74\n\t"
        "s_waitcnt lgkmcnt(3)\n\t"
        "v_mfma_f32_32x32x16_bf16 v[96:111], v[144:147], v[128:131], v[96:111]\n\t"
        "v_exp_f32 v75, v75\n\t"
        "v_exp_f32 v76, v76\n\t"
        "v_exp_f32 v77, v77\n\t"
        "v_exp_f32 v78, v78\n\t"
        "v_exp_f32 v79, v79\n\t"
        "v_cvt_pk_bf16_f32 v132, v72, v73\n\t"
        "v_cvt_pk_bf16_f32 v133, v74, v75\n\t"
        "v_cvt_pk_bf16_f32 v134, v76, v77\n\t"
        "v_cvt_pk_bf16_f32 v135, v78, v79\n\t"
        "s_waitcnt lgkmcnt(2)\n\t"
        "v_mfma_f32_32x32x16_bf16 v[64:79], v[136:139], %[q], v[112:127]\n\t"
        "ds_read_b128 v[140:143], %[ka] offset:3072\n\t"
        "v_exp_f32 v80, v80\n\t"
        "v_exp_f32 v81, v81\n\t"
        "v_exp_f32 v82, v82\n\t"
        "v_exp_f32 v83, v83\n\t"
        "v_exp_f32 v84, v84\n\t"
        "v_exp_f32 v85, v85\n\t"
        "v_mfma_f32_32x32x16_bf16 v[96:111], v[148:151], v[132:135], v[96:111]\n\t"
        "s_mov_b64 exec, %[vm]\n\t"
        "ds_read_b128 v[144:147], %[va] offset:2048\n\t"
        "ds_read_b128 v[148:151], %[va] offset:2560\n\t"
        "s_mov_b64 exec, -1\n\t"
        "v_exp_f32 v86, v86\n\t"
        "v_exp_f32 v87, v87\n\t"
        "v_cvt_pk_bf16_f32 v128, v80, v81\n\t"
        "v_cvt_pk_bf16_f32 v129, v82, v83\n\t"
        "v_cvt_pk_bf16_f32 v130, v84, v85\n\t"
        "v_cvt_pk_bf16_f32 v131, v86, v87\n\t"
        "v_exp_f32 v88, v88\n\t"
        "v_exp_f32 v89, v89\n\t"
        "v_exp_f32 v90, v90\n\t"
        "s_waitcnt lgkmcnt(3)\n\t"
        "v_mfma_f32_32x32x16_bf16 v[96:111], v[152:155], v[128:131], v[96:111]\n\t"
        "v_exp_f32 v91, v91\n\t"
        "v_exp_f32 v92, v92\n\t"
        "v_exp_f32 v93, v93\n\t"
        "v_exp_f32 v94, v94\n\t"
        "v_exp_f32 v95, v95\n\t"
        "v_cvt_pk_bf16_f32 v132, v88, v89\n\t"
        "v_cvt_pk_bf16_f32 v133, v90, v91\n\t"
        "v_cvt_pk_bf16_f32 v134, v92, v93\n\t"
        "v_cvt_pk_bf16_f32 v135, v94, v95\n\t"
        "s_waitcnt lgkmcnt(2)\n\t"
        "v_mfma_f32_32x32x16_bf16 v[80:95], v[140:143], %[q], v[112:127]\n\t"
        "ds_read_b128 v[136:139], %[ka] offset:4096\n\t"
        "v_exp_f32 v64, v64\n\t"
        "v_exp_f32 v65, v65\n\t"
        "v_exp_f32 v66, v66\n\t"
        "v_exp_f32 v67, v67\n\t"
        "v_exp_f32 v68, v68\n\t"
        "v_exp_f32 v69, v69\n\t"
        "v_mfma_f32_32x32x16_bf16 v[96:111], v[156:159], v[132:135], v[96:111]\n\t"
        "s_mov_b64 exec, %[vm]\n\t"
        "ds_read_b128 v[152:155], %[va] offset:3072\n\t"
        "ds_read_b128 v[156:159], %[va] offset:3584\n\t"
        "s_mov_b64 exec, -1\n\t"
        "v_exp_f32 v70, v70\n\t"
        "v_exp_f32 v71, v71\n\t"
        "v_cvt_pk_bf16_f32 v128, v64, v65\n\t"
        "v_cvt_pk_bf16_f32 v129, v66, v67\n\t"
        "v_cvt_pk_bf16_f32 v130, v68, v69\n\t"
        "v_cvt_pk_bf16_f32 v131, v70, v71\n\t"
        "v_exp_f32 v72, v72\n\t"
        "v_exp_f32 v73, v73\n\t"
        "v_exp_f32 v74, v74\n\t"
        "s_waitcnt lgkmcnt(3)\n\t"
        "v_mfma_f32_32x32x16_bf16 v[96:111], v[144:147], v[128:131], v[96:111]\n\t"
        "v_exp_f32 v75, v75\n\t"
        "v_exp_f32 v76, v76\n\t"
        "v_exp_f32 v77, v77\n\t"
        "v_exp_f32 v78, v78\n\t"
        "v_exp_f32 v79, v79\n\t"
        "v_cvt_pk_bf16_f32 v132, v72, v73\n\t"
        "v_cvt_pk_bf16_f32 v133, v74, v75\n\t"
        "v_cvt_pk_bf16_f32 v134, v76, v77\n\t"
        "v_cvt_pk_bf16_f32 v135, v78, v79\n\t"
        "s_waitcnt lgkmcnt(2)\n\t"
        "v_mfma_f32_32x32x16_bf16 v[64:79], v[136:139], %[q], v[112:127]\n\t"
        "ds_read_b128 v[140:143], %[ka] offset:5120\n\t"
        "v_exp_f32 v80, v80\n\t"
        "v_exp_f32 v81, v81\n\t"
        "v_exp_f32 v82, v82\n\t"
        "v_exp_f32 v83, v83\n\t"
        "v_exp_f32 v84, v84\n\t"
        "v_exp_f32 v85, v85\n\t"
        "v_mfma_f32_32x32x16_bf16 v[96:111], v[148:151], v[132:135], v[96:111]\n\t"
        "s_mov_b64 exec, %[vm]\n\t"
        "ds_read_b128 v[144:147], %[va] offset:4096\n\t"
        "ds_read_b128 v[148:151], %[va] offset:4608\n\t"
        "s_mov_b64 exec, -1\n\t"
        "v_exp_f32 v86, v86\n\t"
        "v_exp_f32 v87, v87\n\t"
        "v_cvt_pk_bf16_f32 v128, v80, v81\n\t"
        "v_cvt_pk_bf16_f32 v129, v82, v83\n\t"
        "v_cvt_pk_bf16_f32 v130, v84, v85\n\t"
        "v_cvt_pk_bf16_f32 v131, v86, v87\n\t"
        "v_exp_f32 v88, v88\n\t"
        "v_exp_f32 v89, v89\n\t"
        "v_exp_f32 v90, v90\n\t"
        "s_waitcnt lgkmcnt(3)\n\t"
        "v_mfma_f32_32x32x16_bf16 v[96:111], v[152:155], v[128:131], v[96:111]\n\t"
        "v_exp_f32 v91, v91\n\t"
        "v_exp_f32 v92, v92\n\t"
        "v_exp_f32 v93, v93\n\t"
        "v_exp_f32 v94, v94\n\t"
        "v_exp_f32 v95, v95\n\t"
        "v_add_u32 %[ka], 0x1000, %[ka]\n\t"
        "v_add_u32 %[va], 0x1000, %[va]\n\t"
        "v_cvt_pk_bf16_f32 v132, v88, v89\n\t"
        "v_cvt_pk_bf16_f32 v133, v90, v91\n\t"
        "v_cvt_pk_bf16_f32 v134, v92, v93\n\t"
        "v_cvt_pk_bf16_f32 v135, v94, v95\n\t"
        "s_add_u32 %[it], %[it], 1\n\t"
        "s_cmp_lt_u32 %[it], 16\n\t"
        "s_cbranch_scc1 1b\n\t"
        "s_branch 9f\n\t"
        "3:\n\t"
        "s_waitcnt vmcnt(0)\n\t"
        "v_mov_b32 v160, 1\n\t"
        "s_mov_b64 exec, 1\n\t"
        "ds_add_u32 %[cn], v160 offset:4\n\t"
        "s_mov_b64 exec, -1\n\t"
        "s_mov_b32 %[sp], 0\n\t"
        "5:\n\t"
        "ds_read_b32 v160, %[cn] offset:4\n\t"
        "s_waitcnt lgkmcnt(0)\n\t"
        "v_readfirstlane_b32 %[st], v160\n\t"
        "s_add_u32 %[sp], %[sp], 1\n\t"
        "s_cmp_ge_u32 %[st], 8\n\t"
        "s_cbranch_scc1 2b\n\t"
        "s_sleep 1\n\t"
        "s_cmp_lt_u32 %[sp], %[cap]\n\t"
        "s_cbranch_scc1 5b\n\t"
        "s_branch 2b\n\t"
        "4:\n\t"
        "s_mov_b32 %[sp], 0\n\t"
        "6:\n\t"
        "ds_read_b32 v160, %[cn] offset:8\n\t"
        "s_waitcnt lgkmcnt(0)\n\t"
        "v_readfirstlane_b32 %[st], v160\n\t"
        "s_add_u32 %[sp], %[sp], 1\n\t"
        "s_cmp_ge_u32 %[st], 4\n\t"
        "s_cbranch_scc1 2b\n\t"
        "s_sleep 2\n\t"
        "s_cmp_lt_u32 %[sp], %[cap]\n\t"
        "s_cbranch_scc1 6b\n\t"
        "s_branch 2b\n\t"
        "9:\n\t"
        "s_nop 1\n\t"
        "v_mfma_f32_32x32x16_bf16 v[96:111], v[156:159], v[132:135], v[96:111]\n\t"
        "s_waitcnt lgkmcnt(0)\n\t"
        "s_nop 15\n\t"
        "v_mov_b32 %[o0], v96\n\t"
        "v_mov_b32 %[o1], v97\n\t"
        "v_mov_b32 %[o2], v98\n\t"
        "v_mov_b32 %[o3], v99\n\t"
        "v_mov_b32 %[o4], v100\n\t"
        "v_mov_b32 %[o5], v101\n\t"
        "v_mov_b32 %[o6], v102\n\t"
        "v_mov_b32 %[o7], v103\n\t"
        "v_mov_b32 %[o8], v104\n\t"
        : [o0] "=&v"(o[0]), [o1] "=&v"(o[1]), [o2] "=&v"(o[2]), [o3] "=&v"(o[3]), [o4] "=&v"(o[4]), [o5] "=&v"(o[5]), [o6] "=&v"(o[6]), [o7] "=&v"(o[7]), [o8] "=&v"(o[8]), [ka] "+v"(ka), [va] "+v"(va), [it] "=&s"(s_it), [sp] "=&s"(s_sp), [st] "=&s"(s_st)
        : [q] "v"(qf), [nm] "v"(nm), [one] "v"(onev), [vm] "s"(0x0000ffff0000ffffull), [cn] "v"(cnt_addr), [cap] "s"(SPIN_CAP), [pd] "i"(PRIO_DROP)
        : "memory", "scc", "v64", "v65", "v66", "v67", "v68", "v69", "v70", "v71", "v72", "v73", "v74", "v75", "v76", "v77", "v78", "v79", "v80", "v81", "v82", "v83", "v84", "v85", "v86", "v87", "v88", "v89", "v90", "v91", "v92", "v93", "v94", "v95", "v96", "v97", "v98", "v99", "v100", "v101", "v102", "v103", "v104", "v105", "v106", "v107", "v108", "v109", "v110", "v111", "v112", "v113", "v114", "v115", "v116", "v117", "v118", "v119", "v120", "v121", "v122", "v123", "v124", "v125", "v126", "v127", "v128", "v129", "v130", "v131", "v132", "v133", "v134", "v135", "v136", "v137", "v138", "v139", "v140", "v141", "v142", "v143", "v144", "v145", "v146", "v147", "v148", "v149", "v150", "v151", "v152", "v153", "v154", "v155", "v156", "v157", "v158", "v159", "v160");
        }
        STAMP(4); CSTAMP(11);
        float big = __builtin_fabsf(o[8]);
#pragma unroll
        for (int i = 0; i < 8; ++i) big = __builtin_fmaxf(big, __builtin_fabsf(o[i]) * 0x1p-60f);
        if (__builtin_expect(__any(!(big < 0x1p60f) || !(o[8] > 0x1p-20f)), 0)) {
            float mr = -INFINITY; f32x16 oo = zero;
            const bf16x8 ones = asfrag((u32x4){0x3f803f80u, 0x3f803f80u, 0x3f803f80u, 0x3f803f80u});
            for (int t = 0; t < 64; ++t) {
                f32x16 s = MFMA(ldfrag(lds + L_K + t * 1024 + lo16), qf, zero);
                const float mn = __builtin_fmaxf(mr, halfmax(max16(s)));
                const float al = __builtin_amdgcn_exp2f(mr - mn); mr = mn;
#pragma unroll
                for (int i = 0; i < 16; ++i) { oo[i] *= al; s[i] = __builtin_amdgcn_exp2f(s[i] - mn); }
                lds_ptr vp = lds + L_V + t * 1024 + hi * 256 + (lane & 15) * 16;
                const bf16x8 v0 = ldfrag(vp), v1 = ldfrag(vp + 512);
                oo = MFMA((lane & 16) ? ones : v0, asfrag(pack8(s, 0)), oo);
                oo = MFMA((lane & 16) ? ones : v1, asfrag(pack8(s, 1)), oo);
            }
#pragma unroll
            for (int i = 0; i < 9; ++i) o[i] = oo[i];
        }
        const float rl = __builtin_amdgcn_rcpf(o[8]);
        gst_dev(ws + WS_O + (size_t)(tile * 32 + head) * 1024, lo16,
                (u32x4){cvtpk(o[0] * rl, o[1] * rl), cvtpk(o[2] * rl, o[3] * rl), cvtpk(o[4] * rl, o[5] * rl), cvtpk(o[6] * rl, o[7] * rl)});
        publish(ws, F_O + (tile * 32 + head) * 8, tag, lane);
        STAMP(5);
        STAMP_FLUSH();
        return;
    }
    const int m = w - 8;
    lds_ptr ring = lds + L_B + m * 8192;
    const unsigned stw = (lane >> 3) * 128 + (((lane & 7) ^ (((lane >> 4) & 1) << 2)) * 16);
    const unsigned trb = ((lane & 15) >> 2) * 128 + (((lane >> 3) & 1) * 64) + ((lane >> 4) & 1) * 32 + (lane & 3) * 8;
    const int cp = (hg >> 1) * 14 + (hg & 1) * 8 + 2 * m;
    const bool hact = m < ((hg & 1) ? 3 : 4);
    const char* w1s = w1 + (hact ? cp : 0) * 64;
    const unsigned w1vo = (lane >> 3) * 3584 + (lane & 7) * 16;
    u32x4 st[16];
    u64 tag;
    float zA, zB;
    f32x16 a00, a01, a10, a11;
    {
        const char* xs = x + (size_t)(64 * tg + 16 * m) * 1024;
        const unsigned xvo = (lane >> 3) * 1024 + (((lane & 7) ^ (lane >> 4)) * 16);
        const char* wsrc = wqkv + (size_t)(16 * m) * 3072 + hg * 128;
        const unsigned wvo = (lane >> 3) * 3072 + (((lane & 7) ^ (((lane >> 4) & 1) << 2)) * 16);
        auto issue = [&](int i) { const int kc = (i + rot) & 7;
            glds16(xs + kc * 128, xvo, l0 + ximg(kc) + m * 2048); glds16(xs + kc * 128 + 8192, xvo ^ 64, l0 + ximg(kc) + m * 2048 + 1024);
#pragma unroll
            for (int sg = 0; sg < 3; ++sg) { const char* p = wsrc + (size_t)kc * 64 * 3072 + sg * 1024; const unsigned d = l0 + wimg((i & 3) * 3 + sg) + m * 2048;
                glds16(p, wvo, d); glds16(p + 8 * 3072, wvo, d + 1024); } };
        auto w1load = [&](int k) { const int kk = (k + 4 * rot) & 31; GLD(st[2 * k], w1s + (size_t)(16 * kk) * 3584, w1vo); GLD(st[2 * k + 1], w1s + (size_t)(16 * kk + 8) * 3584, w1vo); };
#pragma unroll
        for (int i = 0; i < 4; ++i) issue(i);
        u64 ep; u32x4 gv[8]; u32x2 bv[8];
        asm volatile("global_load_dwordx2 %0, %1, %2 sc1" : "=v"(ep) : "v"(0u), "s"(ws + F_EPOCH) : "memory");
#pragma unroll
        for (int i = 0; i < 8; ++i) GLD(gv[i], gw + (((i + rot) & 7) * 64 + 16 * m) * 2, hi * 16);
#pragma unroll
        for (int g = 0; g < 8; ++g) asm volatile("global_load_dwordx2 %0, %1, %2" : "=v"(bv[g]) : "v"(hi * 8), "s"(b1 + (hact ? cp : 0) * 16 + 4 * g) : "memory");
        float pA = 0.f, pB = 0.f;
        auto gate = [&](int i) {
            const unsigned xb = (ximg((i + rot) & 7) + xfo) ^ (m * 32);
            const u32x4 xa = __builtin_bit_cast(u32x4, ldfrag(lds + xb)), xc = __builtin_bit_cast(u32x4, ldfrag(lds + xb + 4096));
#pragma unroll
            for (int j = 0; j < 4; ++j) {
                const unsigned ua = xa[j], uc = xc[j], ug = gv[i][j];
                pA = __builtin_amdgcn_fdot2_f32_bf16(__builtin_bit_cast(bf16x2, ua), __builtin_bit_cast(bf16x2, ug), pA, false);
                pB = __builtin_amdgcn_fdot2_f32_bf16(__builtin_bit_cast(bf16x2, uc), __builtin_bit_cast(bf16x2, ug), pB, false);
            }
            asm volatile("" : "+v"(pA), "+v"(pB)); };
        constexpr int WCNT[8] = {41, 33, 33, 33, 16, 16, 12, 10};
#pragma unroll
        for (int i = 0; i < 8; ++i) {
            if (i == 4) asm volatile("s_waitcnt vmcnt(16)" : "+v"(ep), "+v"(gv[0]), "+v"(gv[1]), "+v"(gv[2]), "+v"(gv[3]), "+v"(gv[4]), "+v"(gv[5]), "+v"(gv[6]), "+v"(gv[7]),
                                     "+v"(bv[0]), "+v"(bv[1]), "+v"(bv[2]), "+v"(bv[3]), "+v"(bv[4]), "+v"(bv[5]), "+v"(bv[6]), "+v"(bv[7]) :: "memory");
            else WAITV(WCNT[i]);
            BARRIER();
            if (i >= 1 && i <= 4) issue(i + 3);
            if (i == 5) { w1load(0); w1load(1); }
            if (i == 6) { w1load(2); w1load(3); w1load(4); }
            if (i == 7) w1load(5);
            if (i >= 4) { gate(2 * i - 8); gate(2 * i - 7); }
        }
#pragma unroll
        for (int g = 0; g < 4; ++g)
#pragma unroll
            for (int j = 0; j < 4; ++j) {
                const unsigned u0 = bv[g][j >> 1], u1 = bv[4 + g][j >> 1];
                a00[4 * g + j] = __uint_as_float((j & 1) ? (u0 & 0xffff0000u) : (u0 << 16));
                a10[4 * g + j] = __uint_as_float((j & 1) ? (u1 & 0xffff0000u) : (u1 << 16));
            }
        *(LAS float*)(ring + 6144 + 512 + lane * 4) = pA; *(LAS float*)(ring + 6144 + 768 + lane * 4) = pB;
        if (m == 0 && lane == 0) { *(LAS u32x4*)(lds + L_MISC) = (u32x4){0u, 0u, 0u, 0u}; *(LAS u64*)(lds + L_MISC + 16) = ep + 1; *(LAS u64*)(lds + L_MISC + 24) = 0; }
        BARRIER();
        tag = *(LAS const u64*)(lds + L_MISC + 16);
        zA = 0.f; zB = 0.f;
#pragma unroll
        for (int q = 0; q < 4; ++q) {
            lds_ptr pp = lds + L_B + q * 8192 + 6144 + 512 + (lane & 31) * 4;
            zA += *(LAS const float*)pp + *(LAS const float*)(pp + 128); zB += *(LAS const float*)(pp + 256) + *(LAS const float*)(pp + 384);
        }
        asm volatile("" : "+v"(zA), "+v"(zB));
    }
    {
        a01 = a00; a11 = a10;
        const unsigned trn = trb + 8 * hi * 128;
        __builtin_amdgcn_s_setprio(2);
        constexpr int HD = 6;
        bf16x8 f0[2], f1[2], xa[2], xc[2];
#pragma unroll
        for (int k = 0; k <= 32; ++k) {
            const int r = k % HD;
            lds_ptr sl = ring + (k & 1) * 2048;
            unsigned xb = 0;
            if (k < 32) {
                asm volatile("s_waitcnt vmcnt(%2)" : "+v"(st[2 * r]), "+v"(st[2 * r + 1]) : "i"(k + HD <= 32 ? 2 * HD - 2 : 2 * (31 - k)) : "memory");
                *(LAS u32x4*)(sl + stw) = st[2 * r]; *(LAS u32x4*)(sl + 1024 + stw) = st[2 * r + 1];
                xb = (ximg(((k >> 2) + rot) & 7) + xfo) ^ ((k & 3) * 32);
            }
            if (k > 0) a00 = MFMA(f0[(k - 1) & 1], xa[(k - 1) & 1], a00);
            if (k < 32) { xa[k & 1] = ldfrag(lds + xb); f0[k & 1] = trfrag(sl + trn, 512); }
            __builtin_amdgcn_sched_barrier(0);
            if (k > 0) a10 = MFMA(f1[(k - 1) & 1], xa[(k - 1) & 1], a10);
            if (k < 32) { f1[k & 1] = trfrag(sl + (trn ^ 64), 512); xc[k & 1] = ldfrag(lds + xb + 4096); }
            __builtin_amdgcn_sched_barrier(0);
            if (k > 0) a01 = MFMA(f0[(k - 1) & 1], xc[(k - 1) & 1], a01);
            if (k + HD < 32) { const int kk = (k + HD + 4 * rot) & 31; GLD(st[2 * r], w1s + (size_t)(16 * kk) * 3584, w1vo); GLD(st[2 * r + 1], w1s + (size_t)(16 * kk + 8) * 3584, w1vo); }
            if (k > 0) a11 = MFMA(f1[(k - 1) & 1], xc[(k - 1) & 1], a11);
            __builtin_amdgcn_sched_barrier(0);
        }
        asm volatile("" : "+v"(a00), "+v"(a01), "+v"(a10), "+v"(a11));
    }
    STAMP(1);
    lds_arrive(lds + L_MISC + 12, lane); lds_wait(lds + L_MISC + 12, 4);
    lds_wait(lds + L_MISC + 24, 1);
    {
        const int head = xcd * 4 + (bi & 3);
        const char* kb = ws + WS_K + (size_t)head * 65536; const char* vb = ws + WS_V + (size_t)head * 65536;
#pragma unroll
        for (int j = 0; j < 8; ++j) { const int t = 32 + 8 * m + j; glds16_dev(kb + t * 1024, lo16, l0 + L_K + t * 1024); glds16_dev(vb + t * 1024, lo16, l0 + L_V + t * 1024); }
    }
    if (hact) {
#pragma unroll
        for (int i = 0; i < 16; ++i) { a00[i] = mish(a00[i]); a10[i] = mish(a10[i]); }
        const char* hd = ws + WS_H + (size_t)(2 * tg * 112 + 2 * cp) * 1024;
        gst_dev(hd, lo16, pack8(a00, 0)); gst_dev(hd + 1024, lo16, pack8(a00, 1)); gst_dev(hd + 2048, lo16, pack8(a10, 0)); gst_dev(hd + 3072, lo16, pack8(a10, 1));
#pragma unroll
        for (int i = 0; i < 16; ++i) { a01[i] = mish(a01[i]); a11[i] = mish(a11[i]); }
        hd += 112 * 1024;
        gst_dev(hd, lo16, pack8(a01, 0)); gst_dev(hd + 1024, lo16, pack8(a01, 1)); gst_dev(hd + 2048, lo16, pack8(a11, 0)); gst_dev(hd + 3072, lo16, pack8(a11, 1));
    }
    publish(ws, F_H + (tg * 32 + hg * 4 + m) * 8, tag, lane);
    lds_arrive(lds + L_MISC + 8, lane);
    STAMP(2);
    int ln = __builtin_amdgcn_mbcnt_hi(~0u, __builtin_amdgcn_mbcnt_lo(~0u, 0u));
    asm volatile("" : "+v"(ln));
    const unsigned w2vo = (ln >> 3) * 1024 + (ln & 7) * 16;
    const unsigned trp = ((ln & 15) >> 2) * 128 + (((ln >> 3) & 1) * 64) + ((ln >> 4) & 1) * 32 + (ln & 3) * 8 + 4 * (ln >> 5) * 128;
    {
        const char* w2s = w2 + (size_t)(28 * m) * 16384 + hg * 128;
        const char* hA = ws + WS_H + (size_t)(2 * tg * 112 + 28 * m) * 1024;
#pragma unroll
        for (int k = 0; k < 4; ++k) { GLD(st[2 * k], w2s + k * 16384, w2vo); GLD(st[2 * k + 1], w2s + k * 16384 + 8192, w2vo); }
        poll_tags(ws, F_H + tg * 256, 32, tag, lane);
        STAMP(3);
#pragma unroll
        for (int k = 0; k < 4; ++k) { GLD_DEV(st[8 + 2 * k], hA + k * 1024, lo16); GLD_DEV(st[9 + 2 * k], hA + (112 + k) * 1024, lo16); }
        WAITV(0);
        a00 = (f32x16){}; a01 = (f32x16){}; a10 = (f32x16){}; a11 = (f32x16){};
        auto bsteps = [&](int it, auto LAST) {
#pragma unroll
            for (int k = 0; k < 4; ++k) {
                asm volatile("s_waitcnt vmcnt(%4)" : "+v"(st[2 * k]), "+v"(st[2 * k + 1]), "+v"(st[8 + 2 * k]), "+v"(st[9 + 2 * k]) : "i"(LAST.value ? 12 - 4 * k : 12) : "memory");
                lds_ptr sl = ring + (k & 1) * 2048;
                *(LAS u32x4*)(sl + stw) = st[2 * k]; *(LAS u32x4*)(sl + 1024 + stw) = st[2 * k + 1];
                const bf16x8 f0 = trfrag(sl + trp, 1024), f1 = trfrag(sl + (trp ^ 64), 1024);
                const bf16x8 xa = asfrag(st[8 + 2 * k]), xc = asfrag(st[9 + 2 * k]);
                a00 = MFMA(f0, xa, a00); PACE(); a01 = MFMA(f0, xc, a01); PACE(); a10 = MFMA(f1, xa, a10); PACE(); a11 = MFMA(f1, xc, a11); PACE();
                if (!LAST.value) { const int kn = 4 * (it + 1) + k;
                    GLD(st[2 * k], w2s + kn * 16384, w2vo); GLD(st[2 * k + 1], w2s + kn * 16384 + 8192, w2vo);
                    GLD_DEV(st[8 + 2 * k], hA + kn * 1024, lo16); GLD_DEV(st[9 + 2 * k], hA + (112 + kn) * 1024, lo16); }
            } };
#pragma unroll 1
        for (int it = 0; it < 6; ++it) bsteps(it, std::false_type{});
        bsteps(6, std::true_type{});
        asm volatile("" : "+v"(a00), "+v"(a01), "+v"(a10), "+v"(a11));
    }
    STAMP(4);
    zA = __builtin_fmaxf(zA, -30.f); zB = __builtin_fmaxf(zB, -30.f);
    const float rhoA = __builtin_amdgcn_exp2f(-zA * LOG2E), rhoB = __builtin_amdgcn_exp2f(-zB * LOG2E);
    bf16x8 wf[10];
    {
        const char* wos = wo + (size_t)(8 * m) * 16384 + hg * 128;
#pragma unroll
        for (int k = 0; k < 8; ++k) { GLD(st[2 * k], wos + k * 16384, w2vo); GLD(st[2 * k + 1], wos + k * 16384 + 8192, w2vo); }
#pragma unroll
        for (int i = 0; i < 16; ++i) { a00[i] *= rhoA; a10[i] *= rhoA; a01[i] *= rhoB; a11[i] *= rhoB; }
        asm volatile("" : "+v"(a00), "+v"(a01), "+v"(a10), "+v"(a11));
#pragma unroll
        for (int k = 0; k < 8; ++k) {
            asm volatile("s_waitcnt vmcnt(%2)" : "+v"(st[2 * k]), "+v"(st[2 * k + 1]) : "i"(14 - 2 * k) : "memory");
            lds_ptr sl = ring + (k < 5 ? (k & 1) : (k - 5)) * 2048;
            *(LAS u32x4*)(sl + stw) = st[2 * k]; *(LAS u32x4*)(sl + 1024 + stw) = st[2 * k + 1];
            if (k < 5) { wf[2 * k] = trfrag(sl + trp, 1024); wf[2 * k + 1] = trfrag(sl + (trp ^ 64), 1024); }
        }
    }
#pragma unroll
    for (int tt = 0; tt < 2; ++tt) {
        poll_tags(ws, F_O + ((2 * tg + tt) * 32 + 8 * m) * 8, 8, tag, lane);
        const char* os = ws + WS_O + (size_t)((2 * tg + tt) * 32 + 8 * m) * 1024;
#pragma unroll
        for (int k = 0; k < 8; ++k) GLD_DEV(st[k], os + k * 1024, lo16);
#pragma unroll
        for (int k = 0; k < 8; ++k) {
            bf16x8 f0, f1;
            if (k < 5) { f0 = wf[2 * k]; f1 = wf[2 * k + 1]; } else { f0 = trfrag(ring + (k - 5) * 2048 + trp, 1024); f1 = trfrag(ring + (k - 5) * 2048 + (trp ^ 64), 1024); }
            asm volatile("s_waitcnt vmcnt(%1)" : "+v"(st[k]) : "i"(7 - k) : "memory");
            if (tt == 0) { a00 = MFMA(f0, asfrag(st[k]), a00); a10 = MFMA(f1, asfrag(st[k]), a10); }
            else         { a01 = MFMA(f0, asfrag(st[k]), a01); a11 = MFMA(f1, asfrag(st[k]), a11); }
        }
        STAMP(5 + tt);
    }
    BARRIER();
    if (b == 0 && tid == 512) __hip_atomic_store((GAS u64*)(ws + F_EPOCH), tag, __ATOMIC_RELAXED, __HIP_MEMORY_SCOPE_AGENT);
    auto put = [&](const f32x16& a, int ti) {
        if (ti != m) {
#pragma unroll
            for (int q = 0; q < 4; ++q) *(LAS f32x4*)(lds + ((ti * 4 + m) * 4 + q) * 1024 + lo16) = (f32x4){a[4 * q], a[4 * q + 1], a[4 * q + 2], a[4 * q + 3]};
        } };
    put(a00, 0); put(a01, 1); put(a10, 2); put(a11, 3);
    BARRIER();
    f32x16 acc = m == 0 ? a00 : m == 1 ? a01 : m == 2 ? a10 : a11;
#pragma unroll
    for (int o = 1; o < 4; ++o) {
        const int src = (m + o) & 3;
#pragma unroll
        for (int q = 0; q < 4; ++q) { const f32x4 v = *(LAS const f32x4*)(lds + ((m * 4 + src) * 4 + q) * 1024 + lo16); acc[4 * q] += v[0]; acc[4 * q + 1] += v[1]; acc[4 * q + 2] += v[2]; acc[4 * q + 3] += v[3]; }
    }
    const float g = __builtin_amdgcn_rcpf(1.f + ((m & 1) ? rhoB : rhoA));
    char* op = out + (size_t)(64 * tg + (m & 1) * 32 + (ln & 31)) * 1024 + hg * 128 + (m >> 1) * 64 + (ln >> 5) * 16;
#pragma unroll
    for (int p = 0; p < 2; ++p) {
        const unsigned a0 = cvtpk(acc[8 * p] * g, acc[8 * p + 1] * g), a1 = cvtpk(acc[8 * p + 2] * g, acc[8 * p + 3] * g);
        const unsigned b0 = cvtpk(acc[8 * p + 4] * g, acc[8 * p + 5] * g), b1v = cvtpk(acc[8 * p + 6] * g, acc[8 * p + 7] * g);
        auto r0 = __builtin_amdgcn_permlane32_swap(a0, b0, false, false);
        auto r1 = __builtin_amdgcn_permlane32_swap(a1, b1v, false, false);
        *(u32x4*)(op + p * 32) = (u32x4){r0[0], r1[0], r0[1], r1[1]};
    }
    STAMP(7);
    STAMP_FLUSH();
}

extern "C" void kernel_launch(void* const* d_in, const int* in_sizes, int n_in, void* d_out, int out_size, void* d_ws, size_t ws_size, hipStream_t stream) {
    (void)hipFuncSetAttribute((const void*)fused_kernel, hipFuncAttributeMaxDynamicSharedMemorySize, LDS_BYTES);
    fused_kernel<<<256, 768, LDS_BYTES, stream>>>((const char*)d_in[0], (const char*)d_in[1], (const char*)d_in[2], (const char*)d_in[4], (const unsigned*)d_in[5],
                                                  (const char*)d_in[6], (const char*)d_in[7], (char*)d_ws, (char*)d_out);
}
```

```cpp
#define DBG_STAMPS
#define ZERO 1
#define GAP 0
#define W0 0
#define W1 8
#define SI 0
#include <hip/hip_runtime.h>
#include <hip/hip_bf16.h>
#include <stdint.h>
#include <type_traits>

using bf16x8 = __attribute__((ext_vector_type(8))) short;
using s16x4  = __attribute__((ext_vector_type(4))) short;
using f32x16 = __attribute__((ext_vector_type(16))) float;
using f32x4  = __attribute__((ext_vector_type(4))) float;
using f32x2  = __attribute__((ext_vector_type(2))) float;
using u32x4  = __attribute__((ext_vector_type(4))) unsigned;
using u32x2  = __attribute__((ext_vector_type(2))) unsigned;
typedef __bf16 bf16x2 __attribute__((ext_vector_type(2)));
typedef short v4i16_t __attribute__((ext_vector_type(4)));
typedef unsigned long long u64;
#define DI __device__ __forceinline__
#define LAS __attribute__((address_space(3)))
#define GAS __attribute__((address_space(1)))
#define MFMA(a, b, c) __builtin_amdgcn_mfma_f32_32x32x16_bf16((a), (b), (c), 0, 0, 0)
typedef LAS char* lds_ptr;

constexpr unsigned WS_Q = 0, WS_K = 2u << 20, WS_V = 4u << 20, WS_O = 6u << 20, WS_H = 8u << 20, WS_F = 16u << 20;
constexpr unsigned WS_BYTES = (16u << 20) + 65536;
constexpr unsigned F_QKV = WS_F, F_H = WS_F + 4096, F_O = WS_F + 16384, F_EPOCH = WS_F + 40960;
constexpr unsigned L_K = 0, L_V = 65536, L_B = 131072, L_MISC = L_B + 6144, LDS_BYTES = 163840;
#define PRIO_DROP 11
constexpr unsigned SPIN_CAP = 1u << 16;
constexpr float LOG2E = 1.4426950408889634f;
constexpr float QSCALE = 0.25f * LOG2E;

DI unsigned cvtpk(float lo, float hi) { return __builtin_bit_cast(unsigned, __builtin_convertvector((f32x2){lo, hi}, bf16x2)); }
DI u32x4 pack8(const f32x16& a, int s) { return (u32x4){cvtpk(a[8*s], a[8*s+1]), cvtpk(a[8*s+2], a[8*s+3]), cvtpk(a[8*s+4], a[8*s+5]), cvtpk(a[8*s+6], a[8*s+7])}; }
DI void glds16(const void* sbase, unsigned voff, unsigned lds_dst) {
    unsigned sv; asm volatile("s_mov_b32 %0, m0\n\ts_mov_b32 m0, %3\n\ts_nop 0\n\tglobal_load_lds_dwordx4 %1, %2\n\ts_mov_b32 m0, %0" : "=&s"(sv) : "v"(voff), "s"(sbase), "s"(lds_dst) : "memory"); }
DI void glds16_dev(const void* sbase, unsigned voff, unsigned lds_dst) {
    unsigned sv; asm volatile("s_mov_b32 %0, m0\n\ts_mov_b32 m0, %3\n\ts_nop 0\n\tglobal_load_lds_dwordx4 %1, %2 sc1\n\ts_mov_b32 m0, %0" : "=&s"(sv) : "v"(voff), "s"(sbase), "s"(lds_dst) : "memory"); }
DI void GLD(u32x4& dst, const void* sbase, unsigned voff) { asm volatile("global_load_dwordx4 %0, %1, %2" : "=v"(dst) : "v"(voff), "s"(sbase) : "memory"); }
DI void GLD_DEV(u32x4& dst, const void* sbase, unsigned voff) { asm volatile("global_load_dwordx4 %0, %1, %2 sc1" : "=v"(dst) : "v"(voff), "s"(sbase) : "memory"); }
DI void gst_dev(const void* sbase, unsigned voff, u32x4 v) { asm volatile("global_store_dwordx4 %0, %1, %2 sc1\n\ts_nop 1" :: "v"(voff), "v"(v), "s"(sbase) : "memory"); }
DI s16x4 trd(lds_ptr p) { return __builtin_bit_cast(s16x4, __builtin_amdgcn_ds_read_tr16_b64_v4i16((LAS v4i16_t*)p)); }
DI bf16x8 trfrag(lds_ptr p, int second) { s16x4 a = trd(p), b = trd(p + second); return __builtin_shufflevector(a, b, 0, 1, 2, 3, 4, 5, 6, 7); }
DI bf16x8 ldfrag(lds_ptr p) { return *(LAS const bf16x8*)p; }
DI bf16x8 asfrag(u32x4 v) { return __builtin_bit_cast(bf16x8, v); }
#define PACE_N 1
#define PACE() do { __builtin_amdgcn_sched_barrier(0); if (PACE_N) __builtin_amdgcn_s_sleep(PACE_N); __builtin_amdgcn_sched_barrier(0); } while (0)
#define WAITV(n) asm volatile("s_waitcnt vmcnt(%0)" :: "i"(n) : "memory")
#define BARRIER() do { asm volatile("" ::: "memory"); __builtin_amdgcn_s_waitcnt(0xC07F); __builtin_amdgcn_s_barrier(); asm volatile("" ::: "memory"); } while (0)
DI float mish(float h) {
    const float u = __builtin_amdgcn_exp2f(__builtin_fminf(h * LOG2E, 40.f));
    const float n = u * (u + 2.f);
    return h * n * __builtin_amdgcn_rcpf(n + 2.f);
}
DI float halfmax(float m) { auto rr = __builtin_amdgcn_permlane32_swap(__float_as_uint(m), __float_as_uint(m), false, false); return __builtin_fmaxf(__uint_as_float(rr[0]), __uint_as_float(rr[1])); }
DI float max16(const f32x16& p) {
    float a = __builtin_fmaxf(__builtin_fmaxf(p[0], p[1]), p[2]), b = __builtin_fmaxf(__builtin_fmaxf(p[3], p[4]), p[5]);
    a = __builtin_fmaxf(__builtin_fmaxf(a, p[6]), p[7]); b = __builtin_fmaxf(__builtin_fmaxf(b, p[8]), p[9]);
    a = __builtin_fmaxf(__builtin_fmaxf(a, p[10]), p[11]); b = __builtin_fmaxf(__builtin_fmaxf(b, p[12]), p[13]);
    a = __builtin_fmaxf(__builtin_fmaxf(a, p[14]), p[15]); return __builtin_fmaxf(a, b); }
DI void publish(char* ws, unsigned off, u64 tag, int lane) {
    asm volatile("s_waitcnt vmcnt(0)" ::: "memory");
    if (lane == 0) __hip_atomic_store((GAS u64*)(ws + off), tag, __ATOMIC_RELAXED, __HIP_MEMORY_SCOPE_AGENT);
}
DI void poll_tags(char* ws, unsigned off, int n, u64 tag, int lane) {
    GAS u64* p = (GAS u64*)(ws + off) + (lane < n ? lane : 0);
#pragma unroll 1
    for (unsigned s = 0; s < SPIN_CAP; ++s) {
        const u64 v = __hip_atomic_load(p, __ATOMIC_RELAXED, __HIP_MEMORY_SCOPE_AGENT);
        if (__all(v == tag)) break;
        __builtin_amdgcn_s_sleep(2);
    }
    asm volatile("" ::: "memory");
}
DI void lds_arrive(lds_ptr c, int lane) {
    asm volatile("s_waitcnt lgkmcnt(0)" ::: "memory");
    if (lane == 0) __hip_atomic_fetch_add((LAS unsigned*)c, 1u, __ATOMIC_RELAXED, __HIP_MEMORY_SCOPE_WORKGROUP);
}
DI void lds_wait(lds_ptr c, unsigned n) {
#pragma unroll 1
    for (unsigned s = 0; s < SPIN_CAP * 8; ++s) {
        const unsigned v = __builtin_amdgcn_readfirstlane(__hip_atomic_load((LAS unsigned*)c, __ATOMIC_RELAXED, __HIP_MEMORY_SCOPE_WORKGROUP));
        if (v >= n) break;
        __builtin_amdgcn_s_sleep(1);
    }
    asm volatile("" ::: "memory");
}
constexpr unsigned WS_ST = 32u << 20;
#define STAMP(i) do { __builtin_amdgcn_sched_barrier(0); asm volatile("s_memrealtime %0\n\ts_waitcnt lgkmcnt(0)" : "=s"(tstamp[i]) :: "memory"); __builtin_amdgcn_sched_barrier(0); } while (0)
#define CSTAMP(i) do { __builtin_amdgcn_sched_barrier(0); asm volatile("s_memtime %0\n\ts_waitcnt lgkmcnt(0)" : "=s"(tstamp[i]) :: "memory"); __builtin_amdgcn_sched_barrier(0); } while (0)
#define STAMP_DECL() u64 tstamp[16] = {}
#define STAMP_FLUSH() do { if (lane == 0) { _Pragma("unroll") for (int i_ = 0; i_ < 16; ++i_) ((u64*)(ws + WS_ST))[(blockIdx.x * 16 + w) * 16 + i_] = tstamp[i_]; } } while (0)
DI unsigned ximg(int kc) { return 32768u + (kc & 3) * 8192u + (kc >> 2) * 65536u; }
constexpr unsigned wimg(int n) { return n < 4 ? L_B + n * 8192u : n < 8 ? L_K + (n - 4) * 8192u : L_V + (n - 8) * 8192u; }

__global__ __launch_bounds__(768, 3) void fused_kernel(const char* __restrict__ x, const char* __restrict__ wqkv, const char* __restrict__ wo,
                                                       const char* __restrict__ w1, const unsigned* __restrict__ b1, const char* __restrict__ w2,
                                                       const char* __restrict__ gw, char* __restrict__ ws, char* __restrict__ out) {
    extern __shared__ __attribute__((aligned(16))) char lds_raw[];
    lds_ptr lds = (lds_ptr)lds_raw;
    const unsigned l0 = (unsigned)(uintptr_t)lds_raw;
    const int tid = threadIdx.x, lane = tid & 63, w = __builtin_amdgcn_readfirstlane(tid >> 6), hi = lane >> 5;
    const int b = blockIdx.x, xcd = b & 7, bi = b >> 3;
    const int tg = (xcd >> 1) * 8 + (bi & 7), hg = (xcd & 1) * 4 + (bi >> 3);
    const int rot = ((bi & 7) + 2 * (bi >> 3)) & 7;
    const unsigned lo16 = lane * 16;
    const unsigned xfo = (lane & 31) * 128 + ((hi ^ ((lane >> 1) & 7)) * 16);
    STAMP_DECL();
    STAMP(0);
    if (w < 8) {
        const int ct = (w < 4) ? (w & 1) : ((w >> 1) & 1);
        const unsigned wfo = (8 * hi + ((lane & 15) >> 2)) * 128 + ((ct ^ ((lane >> 3) & 1)) * 64) + ((lane >> 4) & 1) * 32 + (lane & 3) * 8;
        f32x16 acc0 = {}, acc1 = {};
        bf16x8 WF[2][4], XA[2][4], XC[2][4];
        if (w < 4) {
            const unsigned sgo = (w >> 1) * 8192u;
#pragma unroll
            for (int i = 0; i <= 8; ++i) {
                unsigned xb = 0, wb = 0;
                if (i < 8) {
                    BARRIER();
                    if (i == 0 || i == 7) STAMP(8 + i);
                    xb = ximg((i + rot) & 7) + xfo;
                    wb = ((i & 3) == 1 ? (w < 2 ? wimg(3) : wimg(4)) : wimg((i & 3) * 3) + sgo) + wfo;
                }
#pragma unroll
                for (int ks = 0; ks < 4; ++ks) {
                    if (i > 0) acc0 = MFMA(WF[(i - 1) & 1][ks], XA[(i - 1) & 1][ks], acc0);
                    if (i < 8) { WF[i & 1][ks] = trfrag(lds + wb + ks * 2048, 512); XA[i & 1][ks] = ldfrag(lds + (xb ^ (ks * 32))); }
                    __builtin_amdgcn_sched_barrier(0);
                    if (i > 0) acc1 = MFMA(WF[(i - 1) & 1][ks], XC[(i - 1) & 1][ks], acc1);
                    if (i < 8) XC[i & 1][ks] = ldfrag(lds + (xb ^ (ks * 32)) + 4096);
                    __builtin_amdgcn_sched_barrier(0);
                }
            }
            if (w < 2) {
#pragma unroll
                for (int i = 0; i < 16; ++i) { acc0[i] *= QSCALE; acc1[i] *= QSCALE; }
            }
            const char* dst = ws + (w < 2 ? WS_Q : WS_K) + (size_t)((4 * hg + 2 * ct) * 64 + 2 * tg) * 1024;
            gst_dev(dst, lo16, pack8(acc0, 0)); gst_dev(dst + 65536, lo16, pack8(acc0, 1));
            gst_dev(dst + 1024, lo16, pack8(acc1, 0)); gst_dev(dst + 65536 + 1024, lo16, pack8(acc1, 1));
        } else {
            const int tt = w & 1;
#pragma unroll
            for (int i = 0; i <= 8; ++i) {
                unsigned xb = 0;
                if (i < 8) {
                    BARRIER();
                    if (i == 0 || i == 7) STAMP(8 + i);
                    xb = ximg((i + rot) & 7) + xfo + tt * 4096;
                }
#pragma unroll
                for (int ks = 0; ks < 4; ++ks) {
                    if (i > 0) acc0 = MFMA(XA[(i - 1) & 1][ks], WF[(i - 1) & 1][ks], acc0);
                    if (i < 8) { WF[i & 1][ks] = trfrag(lds + wimg((i & 3) * 3 + 2) + wfo + ks * 2048, 512); XA[i & 1][ks] = ldfrag(lds + (xb ^ (ks * 32))); }
                    __builtin_amdgcn_sched_barrier(0);
                }
            }
            const char* dst = ws + WS_V + (size_t)((4 * hg + 2 * ct) * 64 + 2 * tg + tt) * 1024;
            const unsigned vo = ((lane >> 4) & 1) * 65536 + hi * 256 + (lane & 15) * 16;
            gst_dev(dst, vo, pack8(acc0, 0)); gst_dev(dst + 512, vo, pack8(acc0, 1));
        }
        STAMP(1);
        WAITV(0); BARRIER();
        STAMP(6);
        const u64 tag = *(LAS const u64*)(lds + L_MISC + 16);
        const int head = xcd * 4 + (bi & 3), r8 = bi >> 2;
        const int tile = 8 * r8 + (w < 4 ? 2 * w : 2 * (w - 4) + 1);
        if (w == 0) {
            if (lane == 0) __hip_atomic_store((GAS u64*)(ws + F_QKV + (hg * 32 + tg) * 8), tag, __ATOMIC_RELAXED, __HIP_MEMORY_SCOPE_AGENT);
            poll_tags(ws, F_QKV + (head >> 2) * 256, 32, tag, lane);
            if (lane == 0) __hip_atomic_store((LAS unsigned*)(lds + L_MISC + 24), 1u, __ATOMIC_RELAXED, __HIP_MEMORY_SCOPE_WORKGROUP);
        } else lds_wait(lds + L_MISC + 24, 1);
        STAMP(2);
        u32x4 qr;
        GLD_DEV(qr, ws + WS_Q + (size_t)(head * 64 + tile) * 1024, lo16);
        const char* kb = ws + WS_K + (size_t)head * 65536; const char* vb = ws + WS_V + (size_t)head * 65536;
#pragma unroll
        for (int j = 0; j < 4; ++j) { glds16_dev(kb + (j * 8 + w) * 1024, lo16, l0 + L_K + (j * 8 + w) * 1024); glds16_dev(vb + (j * 8 + w) * 1024, lo16, l0 + L_V + (j * 8 + w) * 1024); }
        asm volatile("s_waitcnt vmcnt(6)" : "+v"(qr) :: "memory");
        const bf16x8 qf = asfrag(qr);
        lds_arrive(lds + L_MISC, lane); lds_wait(lds + L_MISC, 8);
        STAMP(3); CSTAMP(10);
        if (w >= 4) __builtin_amdgcn_s_setprio(1);
        const f32x16 zero = {};
        const float nm = -halfmax(max16(MFMA(ldfrag(lds + L_K + lo16), qf, zero)));
        float o[9];
        {
            unsigned ka = l0 + L_K + lo16, va = l0 + L_V + hi * 256 + (lane & 15) * 16; const unsigned cnt_addr = l0 + L_MISC;
            unsigned s_it, s_sp, s_st;
            const unsigned onev = ((lane & 27) == 16) ? 0x3f803f80u : 0u;
    asm volatile(
        "ds_read_b128 v[136:139], %[ka]\n\t"
        "ds_read_b128 v[140:143], %[ka] offset:1024\n\t"
        "v_mov_b32 v144, %[one]\n\t"
        "v_mov_b32 v148, %[one]\n\t"
        "v_mov_b32 v145, %[one]\n\t"
        "v_mov_b32 v149, %[one]\n\t"
        "v_mov_b32 v146, %[one]\n\t"
        "v_mov_b32 v150, %[one]\n\t"
        "v_mov_b32 v147, %[one]\n\t"
        "v_mov_b32 v151, %[one]\n\t"
        "v_mov_b32 v152, %[one]\n\t"
        "v_mov_b32 v156, %[one]\n\t"
        "v_mov_b32 v153, %[one]\n\t"
        "v_mov_b32 v157, %[one]\n\t"
        "v_mov_b32 v154, %[one]\n\t"
        "v_mov_b32 v158, %[one]\n\t"
        "v_mov_b32 v155, %[one]\n\t"
        "v_mov_b32 v159, %[one]\n\t"
        "s_mov_b64 exec, %[vm]\n\t"
        "ds_read_b128 v[144:147], %[va] offset:0\n\t"
        "ds_read_b128 v[148:151], %[va] offset:512\n\t"
        "s_mov_b64 exec, -1\n\t"
        "v_mov_b32 v112, %[nm]\n\t"
        "v_mov_b32 v113, %[nm]\n\t"
        "v_mov_b32 v114, %[nm]\n\t"
        "v_mov_b32 v115, %[nm]\n\t"
        "v_mov_b32 v116, %[nm]\n\t"
        "v_mov_b32 v117, %[nm]\n\t"
        "v_mov_b32 v118, %[nm]\n\t"
        "v_mov_b32 v119, %[nm]\n\t"
        "v_mov_b32 v120, %[nm]\n\t"
        "v_mov_b32 v121, %[nm]\n\t"
        "v_mov_b32 v122, %[nm]\n\t"
        "v_mov_b32 v123, %[nm]\n\t"
        "v_mov_b32 v124, %[nm]\n\t"
        "v_mov_b32 v125, %[nm]\n\t"
        "v_mov_b32 v126, %[nm]\n\t"
        "v_mov_b32 v127, %[nm]\n\t"
        "v_mov_b32 v96, 0\n\t"
        "v_mov_b32 v97, 0\n\t"
        "v_mov_b32 v98, 0\n\t"
        "v_mov_b32 v99, 0\n\t"
        "v_mov_b32 v100, 0\n\t"
        "v_mov_b32 v101, 0\n\t"
        "v_mov_b32 v102, 0\n\t"
        "v_mov_b32 v103, 0\n\t"
        "v_mov_b32 v104, 0\n\t"
        "v_mov_b32 v105, 0\n\t"
        "v_mov_b32 v106, 0\n\t"
        "v_mov_b32 v107, 0\n\t"
        "v_mov_b32 v108, 0\n\t"
        "v_mov_b32 v109, 0\n\t"
        "v_mov_b32 v110, 0\n\t"
        "v_mov_b32 v111, 0\n\t"
        "v_mov_b32 v132, 0\n\t"
        "v_mov_b32 v133, 0\n\t"
        "v_mov_b32 v134, 0\n\t"
        "v_mov_b32 v135, 0\n\t"
        "s_mov_b32 %[it], 0\n\t"
        "s_waitcnt lgkmcnt(3)\n\t"
        "v_mfma_f32_32x32x16_bf16 v[64:79], v[136:139], %[q], v[112:127]\n\t"
        "1:\n\t"
        "s_cmp_eq_u32 %[it], 1\n\t"
        "s_cbranch_scc1 3f\n\t"
        "s_cmp_eq_u32 %[it], 7\n\t"
        "s_cbranch_scc1 4f\n\t"
        "s_cmp_lg_u32 %[it], %[pd]\n\t"
        "s_cbranch_scc1 2f\n\t"
        "s_setprio 0\n\t"
        "2:\n\t"
        "s_waitcnt lgkmcnt(2)\n\t"
        "v_mfma_f32_32x32x16_bf16 v[80:95], v[140:143], %[q], v[112:127]\n\t"
        "ds_read_b128 v[136:139], %[ka] offset:2048\n\t"
        "v_exp_f32 v64, v64\n\t"
        "v_exp_f32 v65, v65\n\t"
        "v_exp_f32 v66, v66\n\t"
        "v_exp_f32 v67, v67\n\t"
        "v_exp_f32 v68, v68\n\t"
        "v_exp_f32 v69, v69\n\t"
        "v_mfma_f32_32x32x16_bf16 v[96:111], v[156:159], v[132:135], v[96:111]\n\t"
        "s_mov_b64 exec, %[vm]\n\t"
        "ds_read_b128 v[152:155], %[va] offset:1024\n\t"
        "ds_read_b128 v[156:159], %[va] offset:1536\n\t"
        "s_mov_b64 exec, -1\n\t"
        "v_exp_f32 v70, v70\n\t"
        "v_exp_f32 v71, v71\n\t"
        "v_cvt_pk_bf16_f32 v128, v64, v65\n\t"
        "v_cvt_pk_bf16_f32 v129, v66, v67\n\t"
        "v_cvt_pk_bf16_f32 v130, v68, v69\n\t"
        "v_cvt_pk_bf16_f32 v131, v70, v71\n\t"
        "v_exp_f32 v72, v72\n\t"
        "v_exp_f32 v73, v73\n\t"
        "v_exp_f32 v74, v74\n\t"
        "s_waitcnt lgkmcnt(3)\n\t"
        "v_mfma_f32_32x32x16_bf16 v[96:111], v[144:147], v[128:131], v[96:111]\n\t"
        "v_exp_f32 v75, v75\n\t"
        "v_exp_f32 v76, v76\n\t"
        "v_exp_f32 v77, v77\n\t"
        "v_exp_f32 v78, v78\n\t"
        "v_exp_f32 v79, v79\n\t"
        "v_cvt_pk_bf16_f32 v132, v72, v73\n\t"
        "v_cvt_pk_bf16_f32 v133, v74, v75\n\t"
        "v_cvt_pk_bf16_f32 v134, v76, v77\n\t"
        "v_cvt_pk_bf16_f32 v135, v78, v79\n\t"
        "s_waitcnt lgkmcnt(2)\n\t"
        "v_mfma_f32_32x32x16_bf16 v[64:79], v[136:139], %[q], v[112:127]\n\t"
        "ds_read_b128 v[140:143], %[ka] offset:3072\n\t"
        "v_exp_f32 v80, v80\n\t"
        "v_exp_f32 v81, v81\n\t"
        "v_exp_f32 v82, v82\n\t"
        "v_exp_f32 v83, v83\n\t"
        "v_exp_f32 v84, v84\n\t"
        "v_exp_f32 v85, v85\n\t"
        "v_mfma_f32_32x32x16_bf16 v[96:111], v[148:151], v[132:135], v[96:111]\n\t"
        "s_mov_b64 exec, %[vm]\n\t"
        "ds_read_b128 v[144:147], %[va] offset:2048\n\t"
        "ds_read_b128 v[148:151], %[va] offset:2560\n\t"
        "s_mov_b64 exec, -1\n\t"
        "v_exp_f32 v86, v86\n\t"
        "v_exp_f32 v87, v87\n\t"
        "v_cvt_pk_bf16_f32 v128, v80, v81\n\t"
        "v_cvt_pk_bf16_f32 v129, v82, v83\n\t"
        "v_cvt_pk_bf16_f32 v130, v84, v85\n\t"
        "v_cvt_pk_bf16_f32 v131, v86, v87\n\t"
        "v_exp_f32 v88, v88\n\t"
        "v_exp_f32 v89, v89\n\t"
        "v_exp_f32 v90, v90\n\t"
        "s_waitcnt lgkmcnt(3)\n\t"
        "v_mfma_f32_32x32x16_bf16 v[96:111], v[152:155], v[128:131], v[96:111]\n\t"
        "v_exp_f32 v91, v91\n\t"
        "v_exp_f32 v92, v92\n\t"
        "v_exp_f32 v93, v93\n\t"
        "v_exp_f32 v94, v94\n\t"
        "v_exp_f32 v95, v95\n\t"
        "v_cvt_pk_bf16_f32 v132, v88, v89\n\t"
        "v_cvt_pk_bf16_f32 v133, v90, v91\n\t"
        "v_cvt_pk_bf16_f32 v134, v92, v93\n\t"
        "v_cvt_pk_bf16_f32 v135, v94, v95\n\t"
        "s_waitcnt lgkmcnt(2)\n\t"
        "v_mfma_f32_32x32x16_bf16 v[80:95], v[140:143], %[q], v[112:127]\n\t"
        "ds_read_b128 v[136:139], %[ka] offset:4096\n\t"
        "v_exp_f32 v64, v64\n\t"
        "v_exp_f32 v65, v65\n\t"
        "v_exp_f32 v66, v66\n\t"
        "v_exp_f32 v67, v67\n\t"
        "v_exp_f32 v68, v68\n\t"
        "v_exp_f32 v69, v69\n\t"
        "v_mfma_f32_32x32x16_bf16 v[96:111], v[156:159], v[132:135], v[96:111]\n\t"
        "s_mov_b64 exec, %[vm]\n\t"
        "ds_read_b128 v[152:155], %[va] offset:3072\n\t"
        "ds_read_b128 v[156:159], %[va] offset:3584\n\t"
        "s_mov_b64 exec, -1\n\t"
        "v_exp_f32 v70, v70\n\t"
        "v_exp_f32 v71, v71\n\t"
        "v_cvt_pk_bf16_f32 v128, v64, v65\n\t"
        "v_cvt_pk_bf16_f32 v129, v66, v67\n\t"
        "v_cvt_pk_bf16_f32 v130, v68, v69\n\t"
        "v_cvt_pk_bf16_f32 v131, v70, v71\n\t"
        "v_exp_f32 v72, v72\n\t"
        "v_exp_f32 v73, v73\n\t"
        "v_exp_f32 v74, v74\n\t"
        "s_waitcnt lgkmcnt(3)\n\t"
        "v_mfma_f32_32x32x16_bf16 v[96:111], v[144:147], v[128:131], v[96:111]\n\t"
        "v_exp_f32 v75, v75\n\t"
        "v_exp_f32 v76, v76\n\t"
        "v_exp_f32 v77, v77\n\t"
        "v_exp_f32 v78, v78\n\t"
        "v_exp_f32 v79, v79\n\t"
        "v_cvt_pk_bf16_f32 v132, v72, v73\n\t"
        "v_cvt_pk_bf16_f32 v133, v74, v75\n\t"
        "v_cvt_pk_bf16_f32 v134, v76, v77\n\t"
        "v_cvt_pk_bf16_f32 v135, v78, v79\n\t"
        "s_waitcnt lgkmcnt(2)\n\t"
        "v_mfma_f32_32x32x16_bf16 v[64:79], v[136:139], %[q], v[112:127]\n\t"
        "ds_read_b128 v[140:143], %[ka] offset:5120\n\t"
        "v_exp_f32 v80, v80\n\t"
        "v_exp_f32 v81, v81\n\t"
        "v_exp_f32 v82, v82\n\t"
        "v_exp_f32 v83, v83\n\t"
        "v_exp_f32 v84, v84\n\t"
        "v_exp_f32 v85, v85\n\t"
        "v_mfma_f32_32x32x16_bf16 v[96:111], v[148:151], v[132:135], v[96:111]\n\t"
        "s_mov_b64 exec, %[vm]\n\t"
        "ds_read_b128 v[144:147], %[va] offset:4096\n\t"
        "ds_read_b128 v[148:151], %[va] offset:4608\n\t"
        "s_mov_b64 exec, -1\n\t"
        "v_exp_f32 v86, v86\n\t"
        "v_exp_f32 v87, v87\n\t"
        "v_cvt_pk_bf16_f32 v128, v80, v81\n\t"
        "v_cvt_pk_bf16_f32 v129, v82, v83\n\t"
        "v_cvt_pk_bf16_f32 v130, v84, v85\n\t"
        "v_cvt_pk_bf16_f32 v131, v86, v87\n\t"
        "v_exp_f32 v88, v88\n\t"
        "v_exp_f32 v89, v89\n\t"
        "v_exp_f32 v90, v90\n\t"
        "s_waitcnt lgkmcnt(3)\n\t"
        "v_mfma_f32_32x32x16_bf16 v[96:111], v[152:155], v[128:131], v[96:111]\n\t"
        "v_exp_f32 v91, v91\n\t"
        "v_exp_f32 v92, v92\n\t"
        "v_exp_f32 v93, v93\n\t"
        "v_exp_f32 v94, v94\n\t"
        "v_exp_f32 v95, v95\n\t"
        "v_add_u32 %[ka], 0x1000, %[ka]\n\t"
        "v_add_u32 %[va], 0x1000, %[va]\n\t"
        "v_cvt_pk_bf16_f32 v132, v88, v89\n\t"
        "v_cvt_pk_bf16_f32 v133, v90, v91\n\t"
        "v_cvt_pk_bf16_f32 v134, v92, v93\n\t"
        "v_cvt_pk_bf16_f32 v135, v94, v95\n\t"
        "s_add_u32 %[it], %[it], 1\n\t"
        "s_cmp_lt_u32 %[it], 16\n\t"
        "s_cbranch_scc1 1b\n\t"
        "s_branch 9f\n\t"
        "3:\n\t"
        "s_waitcnt vmcnt(0)\n\t"
        "v_mov_b32 v160, 1\n\t"
        "s_mov_b64 exec, 1\n\t"
        "ds_add_u32 %[cn], v160 offset:4\n\t"
        "s_mov_b64 exec, -1\n\t"
        "s_mov_b32 %[sp], 0\n\t"
        "5:\n\t"
        "ds_read_b32 v160, %[cn] offset:4\n\t"
        "s_waitcnt lgkmcnt(0)\n\t"
        "v_readfirstlane_b32 %[st], v160\n\t"
        "s_add_u32 %[sp], %[sp], 1\n\t"
        "s_cmp_ge_u32 %[st], 8\n\t"
        "s_cbranch_scc1 2b\n\t"
        "s_sleep 1\n\t"
        "s_cmp_lt_u32 %[sp], %[cap]\n\t"
        "s_cbranch_scc1 5b\n\t"
        "s_branch 2b\n\t"
        "4:\n\t"
        "s_mov_b32 %[sp], 0\n\t"
        "6:\n\t"
        "ds_read_b32 v160, %[cn] offset:8\n\t"
        "s_waitcnt lgkmcnt(0)\n\t"
        "v_readfirstlane_b32 %[st], v160\n\t"
        "s_add_u32 %[sp], %[sp], 1\n\t"
        "s_cmp_ge_u32 %[st], 4\n\t"
        "s_cbranch_scc1 2b\n\t"
        "s_sleep 2\n\t"
        "s_cmp_lt_u32 %[sp], %[cap]\n\t"
        "s_cbranch_scc1 6b\n\t"
        "s_branch 2b\n\t"
        "9:\n\t"
        "s_nop 1\n\t"
        "v_mfma_f32_32x32x16_bf16 v[96:111], v[156:159], v[132:135], v[96:111]\n\t"
        "s_waitcnt lgkmcnt(0)\n\t"
        "s_nop 15\n\t"
        "v_mov_b32 %[o0], v96\n\t"
        "v_mov_b32 %[o1], v97\n\t"
        "v_mov_b32 %[o2], v98\n\t"
        "v_mov_b32 %[o3], v99\n\t"
        "v_mov_b32 %[o4], v100\n\t"
        "v_mov_b32 %[o5], v101\n\t"
        "v_mov_b32 %[o6], v102\n\t"
        "v_mov_b32 %[o7], v103\n\t"
        "v_mov_b32 %[o8], v104\n\t"
        : [o0] "=&v"(o[0]), [o1] "=&v"(o[1]), [o2] "=&v"(o[2]), [o3] "=&v"(o[3]), [o4] "=&v"(o[4]), [o5] "=&v"(o[5]), [o6] "=&v"(o[6]), [o7] "=&v"(o[7]), [o8] "=&v"(o[8]), [ka] "+v"(ka), [va] "+v"(va), [it] "=&s"(s_it), [sp] "=&s"(s_sp), [st] "=&s"(s_st)
        : [q] "v"(qf), [nm] "v"(nm), [one] "v"(onev), [vm] "s"(0x0000ffff0000ffffull), [cn] "v"(cnt_addr), [cap] "s"(SPIN_CAP), [pd] "i"(PRIO_DROP)
        : "memory", "scc", "v64", "v65", "v66", "v67", "v68", "v69", "v70", "v71", "v72", "v73", "v74", "v75", "v76", "v77", "v78", "v79", "v80", "v81", "v82", "v83", "v84", "v85", "v86", "v87", "v88", "v89", "v90", "v91", "v92", "v93", "v94", "v95", "v96", "v97", "v98", "v99", "v100", "v101", "v102", "v103", "v104", "v105", "v106", "v107", "v108", "v109", "v110", "v111", "v112", "v113", "v114", "v115", "v116", "v117", "v118", "v119", "v120", "v121", "v122", "v123", "v124", "v125", "v126", "v127", "v128", "v129", "v130", "v131", "v132", "v133", "v134", "v135", "v136", "v137", "v138", "v139", "v140", "v141", "v142", "v143", "v144", "v145", "v146", "v147", "v148", "v149", "v150", "v151", "v152", "v153", "v154", "v155", "v156", "v157", "v158", "v159", "v160");
        }
        STAMP(4); CSTAMP(11);
        float big = __builtin_fabsf(o[8]);
#pragma unroll
        for (int i = 0; i < 8; ++i) big = __builtin_fmaxf(big, __builtin_fabsf(o[i]) * 0x1p-60f);
        if (__builtin_expect(__any(!(big < 0x1p60f) || !(o[8] > 0x1p-20f)), 0)) {
            float mr = -INFINITY; f32x16 oo = zero;
            const bf16x8 ones = asfrag((u32x4){0x3f803f80u, 0x3f803f80u, 0x3f803f80u, 0x3f803f80u});
            for (int t = 0; t < 64; ++t) {
                f32x16 s = MFMA(ldfrag(lds + L_K + t * 1024 + lo16), qf, zero);
                const float mn = __builtin_fmaxf(mr, halfmax(max16(s)));
                const float al = __builtin_amdgcn_exp2f(mr - mn); mr = mn;
#pragma unroll
                for (int i = 0; i < 16; ++i) { oo[i] *= al; s[i] = __builtin_amdgcn_exp2f(s[i] - mn); }
                lds_ptr vp = lds + L_V + t * 1024 + hi * 256 + (lane & 15) * 16;
                const bf16x8 v0 = ldfrag(vp), v1 = ldfrag(vp + 512);
                oo = MFMA((lane & 16) ? ones : v0, asfrag(pack8(s, 0)), oo);
                oo = MFMA((lane & 16) ? ones : v1, asfrag(pack8(s, 1)), oo);
            }
#pragma unroll
            for (int i = 0; i < 9; ++i) o[i] = oo[i];
        }
        const float rl = __builtin_amdgcn_rcpf(o[8]);
        gst_dev(ws + WS_O + (size_t)(tile * 32 + head) * 1024, lo16,
                (u32x4){cvtpk(o[0] * rl, o[1] * rl), cvtpk(o[2] * rl, o[3] * rl), cvtpk(o[4] * rl, o[5] * rl), cvtpk(o[6] * rl, o[7] * rl)});
        publish(ws, F_O + (tile * 32 + head) * 8, tag, lane);
        STAMP(5);
        STAMP_FLUSH();
        return;
    }
    const int m = w - 8;
    lds_ptr ring = lds + L_B + m * 8192;
    const unsigned stw = (lane >> 3) * 128 + (((lane & 7) ^ (((lane >> 4) & 1) << 2)) * 16);
    const unsigned trb = ((lane & 15) >> 2) * 128 + (((lane >> 3) & 1) * 64) + ((lane >> 4) & 1) * 32 + (lane & 3) * 8;
    const int cp = (hg >> 1) * 14 + (hg & 1) * 8 + 2 * m;
    const bool hact = m < ((hg & 1) ? 3 : 4);
    const char* w1s = w1 + (hact ? cp : 0) * 64;
    const unsigned w1vo = (lane >> 3) * 3584 + (lane & 7) * 16;
    u32x4 st[16];
    u64 tag;
    float zA, zB;
    f32x16 a00, a01, a10, a11;
    {
        const char* xs = x + (size_t)(64 * tg + 16 * m) * 1024;
        const unsigned xvo = (lane >> 3) * 1024 + (((lane & 7) ^ (lane >> 4)) * 16);
        const char* wsrc = wqkv + (size_t)(16 * m) * 3072 + hg * 128;
        const unsigned wvo = (lane >> 3) * 3072 + (((lane & 7) ^ (((lane >> 4) & 1) << 2)) * 16);
        auto issue = [&](int i) { const int kc = (i + rot) & 7;
            glds16(xs + kc * 128, xvo, l0 + ximg(kc) + m * 2048); glds16(xs + kc * 128 + 8192, xvo ^ 64, l0 + ximg(kc) + m * 2048 + 1024);
#pragma unroll
            for (int sg = 0; sg < 3; ++sg) { const char* p = wsrc + (size_t)kc * 64 * 3072 + sg * 1024; const unsigned d = l0 + wimg((i & 3) * 3 + sg) + m * 2048;
                glds16(p, wvo, d); glds16(p + 8 * 3072, wvo, d + 1024); } };
        auto w1load = [&](int k) { const int kk = (k + 4 * rot) & 31; GLD(st[2 * k], w1s + (size_t)(16 * kk) * 3584, w1vo); GLD(st[2 * k + 1], w1s + (size_t)(16 * kk + 8) * 3584, w1vo); };
#pragma unroll
        for (int i = 0; i < 4; ++i) issue(i);
        u64 ep; u32x4 gv[8]; u32x2 bv[8];
        asm volatile("global_load_dwordx2 %0, %1, %2 sc1" : "=v"(ep) : "v"(0u), "s"(ws + F_EPOCH) : "memory");
#pragma unroll
        for (int i = 0; i < 8; ++i) GLD(gv[i], gw + (((i + rot) & 7) * 64 + 16 * m) * 2, hi * 16);
#pragma unroll
        for (int g = 0; g < 8; ++g) asm volatile("global_load_dwordx2 %0, %1, %2" : "=v"(bv[g]) : "v"(hi * 8), "s"(b1 + (hact ? cp : 0) * 16 + 4 * g) : "memory");
        float pA = 0.f, pB = 0.f;
        auto gate = [&](int i) {
            const unsigned xb = (ximg((i + rot) & 7) + xfo) ^ (m * 32);
            const u32x4 xa = __builtin_bit_cast(u32x4, ldfrag(lds + xb)), xc = __builtin_bit_cast(u32x4, ldfrag(lds + xb + 4096));
#pragma unroll
            for (int j = 0; j < 4; ++j) {
                const unsigned ua = xa[j], uc = xc[j], ug = gv[i][j];
                pA = __builtin_amdgcn_fdot2_f32_bf16(__builtin_bit_cast(bf16x2, ua), __builtin_bit_cast(bf16x2, ug), pA, false);
                pB = __builtin_amdgcn_fdot2_f32_bf16(__builtin_bit_cast(bf16x2, uc), __builtin_bit_cast(bf16x2, ug), pB, false);
            }
            asm volatile("" : "+v"(pA), "+v"(pB)); };
        constexpr int WCNT[8] = {41, 33, 33, 33, 16, 16, 12, 10};
#pragma unroll
        for (int i = 0; i < 8; ++i) {
            if (i == 4) asm volatile("s_waitcnt vmcnt(16)" : "+v"(ep), "+v"(gv[0]), "+v"(gv[1]), "+v"(gv[2]), "+v"(gv[3]), "+v"(gv[4]), "+v"(gv[5]), "+v"(gv[6]), "+v"(gv[7]),
                                     "+v"(bv[0]), "+v"(bv[1]), "+v"(bv[2]), "+v"(bv[3]), "+v"(bv[4]), "+v"(bv[5]), "+v"(bv[6]), "+v"(bv[7]) :: "memory");
            else WAITV(WCNT[i]);
            BARRIER();
            if (i >= 1 && i <= 4) issue(i + 3);
            if (i == 5) { w1load(0); w1load(1); }
            if (i == 6) { w1load(2); w1load(3); w1load(4); }
            if (i == 7) w1load(5);
            if (i >= 4) { gate(2 * i - 8); gate(2 * i - 7); }
        }
#pragma unroll
        for (int g = 0; g < 4; ++g)
#pragma unroll
            for (int j = 0; j < 4; ++j) {
                const unsigned u0 = bv[g][j >> 1], u1 = bv[4 + g][j >> 1];
                a00[4 * g + j] = __uint_as_float((j & 1) ? (u0 & 0xffff0000u) : (u0 << 16));
                a10[4 * g + j] = __uint_as_float((j & 1) ? (u1 & 0xffff0000u) : (u1 << 16));
            }
        *(LAS float*)(ring + 6144 + 512 + lane * 4) = pA; *(LAS float*)(ring + 6144 + 768 + lane * 4) = pB;
        if (m == 0 && lane == 0) { *(LAS u32x4*)(lds + L_MISC) = (u32x4){0u, 0u, 0u, 0u}; *(LAS u64*)(lds + L_MISC + 16) = ep + 1; *(LAS u64*)(lds + L_MISC + 24) = 0; }
        BARRIER();
        tag = *(LAS const u64*)(lds + L_MISC + 16);
        zA = 0.f; zB = 0.f;
#pragma unroll
        for (int q = 0; q < 4; ++q) {
            lds_ptr pp = lds + L_B + q * 8192 + 6144 + 512 + (lane & 31) * 4;
            zA += *(LAS const float*)pp + *(LAS const float*)(pp + 128); zB += *(LAS const float*)(pp + 256) + *(LAS const float*)(pp + 384);
        }
        asm volatile("" : "+v"(zA), "+v"(zB));
    }
    {
        a01 = a00; a11 = a10;
        const unsigned trn = trb + 8 * hi * 128;
        __builtin_amdgcn_s_setprio(2);
        constexpr int HD = 6;
        bf16x8 f0[2], f1[2], xa[2], xc[2];
#pragma unroll
        for (int k = 0; k <= 32; ++k) {
            const int r = k % HD;
            lds_ptr sl = ring + (k & 1) * 2048;
            unsigned xb = 0;
            if (k < 32) {
                asm volatile("s_waitcnt vmcnt(%2)" : "+v"(st[2 * r]), "+v"(st[2 * r + 1]) : "i"(k + HD <= 32 ? 2 * HD - 2 : 2 * (31 - k)) : "memory");
                *(LAS u32x4*)(sl + stw) = st[2 * r]; *(LAS u32x4*)(sl + 1024 + stw) = st[2 * r + 1];
                xb = (ximg(((k >> 2) + rot) & 7) + xfo) ^ ((k & 3) * 32);
            }
            if (k > 0) a00 = MFMA(f0[(k - 1) & 1], xa[(k - 1) & 1], a00);
            if (k < 32) { xa[k & 1] = ldfrag(lds + xb); f0[k & 1] = trfrag(sl + trn, 512); }
            __builtin_amdgcn_sched_barrier(0);
            if (k > 0) a10 = MFMA(f1[(k - 1) & 1], xa[(k - 1) & 1], a10);
            if (k < 32) { f1[k & 1] = trfrag(sl + (trn ^ 64), 512); xc[k & 1] = ldfrag(lds + xb + 4096); }
            __builtin_amdgcn_sched_barrier(0);
            if (k > 0) a01 = MFMA(f0[(k - 1) & 1], xc[(k - 1) & 1], a01);
            if (k + HD < 32) { const int kk = (k + HD + 4 * rot) & 31; GLD(st[2 * r], w1s + (size_t)(16 * kk) * 3584, w1vo); GLD(st[2 * r + 1], w1s + (size_t)(16 * kk + 8) * 3584, w1vo); }
            if (k > 0) a11 = MFMA(f1[(k - 1) & 1], xc[(k - 1) & 1], a11);
            __builtin_amdgcn_sched_barrier(0);
        }
        asm volatile("" : "+v"(a00), "+v"(a01), "+v"(a10), "+v"(a11));
    }
    STAMP(1);
    lds_arrive(lds + L_MISC + 12, lane); lds_wait(lds + L_MISC + 12, 4);
    lds_wait(lds + L_MISC + 24, 1);
    {
        const int head = xcd * 4 + (bi & 3);
        const char* kb = ws + WS_K + (size_t)head * 65536; const char* vb = ws + WS_V + (size_t)head * 65536;
#pragma unroll
        for (int j = 0; j < 8; ++j) { const int t = 32 + 8 * m + j; glds16_dev(kb + t * 1024, lo16, l0 + L_K + t * 1024); glds16_dev(vb + t * 1024, lo16, l0 + L_V + t * 1024); }
    }
    if (hact) {
#pragma unroll
        for (int i = 0; i < 16; ++i) { a00[i] = mish(a00[i]); a10[i] = mish(a10[i]); }
        const char* hd = ws + WS_H + (size_t)(2 * tg * 112 + 2 * cp) * 1024;
        gst_dev(hd, lo16, pack8(a00, 0)); gst_dev(hd + 1024, lo16, pack8(a00, 1)); gst_dev(hd + 2048, lo16, pack8(a10, 0)); gst_dev(hd + 3072, lo16, pack8(a10, 1));
#pragma unroll
        for (int i = 0; i < 16; ++i) { a01[i] = mish(a01[i]); a11[i] = mish(a11[i]); }
        hd += 112 * 1024;
        gst_dev(hd, lo16, pack8(a01, 0)); gst_dev(hd + 1024, lo16, pack8(a01, 1)); gst_dev(hd + 2048, lo16, pack8(a11, 0)); gst_dev(hd + 3072, lo16, pack8(a11, 1));
    }
    publish(ws, F_H + (tg * 32 + hg * 4 + m) * 8, tag, lane);
    lds_arrive(lds + L_MISC + 8, lane);
    STAMP(2);
    int ln = __builtin_amdgcn_mbcnt_hi(~0u, __builtin_amdgcn_mbcnt_lo(~0u, 0u));
    asm volatile("" : "+v"(ln));
    const unsigned w2vo = (ln >> 3) * 1024 + (ln & 7) * 16;
    const unsigned trp = ((ln & 15) >> 2) * 128 + (((ln >> 3) & 1) * 64) + ((ln >> 4) & 1) * 32 + (ln & 3) * 8 + 4 * (ln >> 5) * 128;
    {
        const char* w2s = w2 + (size_t)(28 * m) * 16384 + hg * 128;
        const char* hA = ws + WS_H + (size_t)(2 * tg * 112 + 28 * m) * 1024;
#pragma unroll
        for (int k = 0; k < 4; ++k) { GLD(st[2 * k], w2s + k * 16384, w2vo); GLD(st[2 * k + 1], w2s + k * 16384 + 8192, w2vo); }
        poll_tags(ws, F_H + tg * 256, 32, tag, lane);
        STAMP(3);
#pragma unroll
        for (int k = 0; k < 4; ++k) { GLD_DEV(st[8 + 2 * k], hA + k * 1024, lo16); GLD_DEV(st[9 + 2 * k], hA + (112 + k) * 1024, lo16); }
        WAITV(0);
        a00 = (f32x16){}; a01 = (f32x16){}; a10 = (f32x16){}; a11 = (f32x16){};
        auto bsteps = [&](int it, auto LAST) {
#pragma unroll
            for (int k = 0; k < 4; ++k) {
                asm volatile("s_waitcnt vmcnt(%4)" : "+v"(st[2 * k]), "+v"(st[2 * k + 1]), "+v"(st[8 + 2 * k]), "+v"(st[9 + 2 * k]) : "i"(LAST.value ? 12 - 4 * k : 12) : "memory");
                lds_ptr sl = ring + (k & 1) * 2048;
                *(LAS u32x4*)(sl + stw) = st[2 * k]; *(LAS u32x4*)(sl + 1024 + stw) = st[2 * k + 1];
                const bf16x8 f0 = trfrag(sl + trp, 1024), f1 = trfrag(sl + (trp ^ 64), 1024);
                const bf16x8 xa = asfrag(st[8 + 2 * k]), xc = asfrag(st[9 + 2 * k]);
                a00 = MFMA(f0, xa, a00); PACE(); a01 = MFMA(f0, xc, a01); PACE(); a10 = MFMA(f1, xa, a10); PACE(); a11 = MFMA(f1, xc, a11); PACE();
                if (!LAST.value) { const int kn = 4 * (it + 1) + k;
                    GLD(st[2 * k], w2s + kn * 16384, w2vo); GLD(st[2 * k + 1], w2s + kn * 16384 + 8192, w2vo);
                    GLD_DEV(st[8 + 2 * k], hA + kn * 1024, lo16); GLD_DEV(st[9 + 2 * k], hA + (112 + kn) * 1024, lo16); }
            } };
#pragma unroll 1
        for (int it = 0; it < 6; ++it) bsteps(it, std::false_type{});
        bsteps(6, std::true_type{});
        asm volatile("" : "+v"(a00), "+v"(a01), "+v"(a10), "+v"(a11));
    }
    STAMP(4);
    zA = __builtin_fmaxf(zA, -30.f); zB = __builtin_fmaxf(zB, -30.f);
    const float rhoA = __builtin_amdgcn_exp2f(-zA * LOG2E), rhoB = __builtin_amdgcn_exp2f(-zB * LOG2E);
    bf16x8 wf[10];
    {
        const char* wos = wo + (size_t)(8 * m) * 16384 + hg * 128;
#pragma unroll
        for (int k = 0; k < 8; ++k) { GLD(st[2 * k], wos + k * 16384, w2vo); GLD(st[2 * k + 1], wos + k * 16384 + 8192, w2vo); }
#pragma unroll
        for (int i = 0; i < 16; ++i) { a00[i] *= rhoA; a10[i] *= rhoA; a01[i] *= rhoB; a11[i] *= rhoB; }
        asm volatile("" : "+v"(a00), "+v"(a01), "+v"(a10), "+v"(a11));
#pragma unroll
        for (int k = 0; k < 8; ++k) {
            asm volatile("s_waitcnt vmcnt(%2)" : "+v"(st[2 * k]), "+v"(st[2 * k + 1]) : "i"(14 - 2 * k) : "memory");
            lds_ptr sl = ring + (k < 5 ? (k & 1) : (k - 5)) * 2048;
            *(LAS u32x4*)(sl + stw) = st[2 * k]; *(LAS u32x4*)(sl + 1024 + stw) = st[2 * k + 1];
            if (k < 5) { wf[2 * k] = trfrag(sl + trp, 1024); wf[2 * k + 1] = trfrag(sl + (trp ^ 64), 1024); }
        }
    }
#pragma unroll
    for (int tt = 0; tt < 2; ++tt) {
        poll_tags(ws, F_O + ((2 * tg + tt) * 32 + 8 * m) * 8, 8, tag, lane);
        const char* os = ws + WS_O + (size_t)((2 * tg + tt) * 32 + 8 * m) * 1024;
#pragma unroll
        for (int k = 0; k < 8; ++k) GLD_DEV(st[k], os + k * 1024, lo16);
#pragma unroll
        for (int k = 0; k < 8; ++k) {
            bf16x8 f0, f1;
            if (k < 5) { f0 = wf[2 * k]; f1 = wf[2 * k + 1]; } else { f0 = trfrag(ring + (k - 5) * 2048 + trp, 1024); f1 = trfrag(ring + (k - 5) * 2048 + (trp ^ 64), 1024); }
            asm volatile("s_waitcnt vmcnt(%1)" : "+v"(st[k]) : "i"(7 - k) : "memory");
            if (tt == 0) { a00 = MFMA(f0, asfrag(st[k]), a00); a10 = MFMA(f1, asfrag(st[k]), a10); }
            else         { a01 = MFMA(f0, asfrag(st[k]), a01); a11 = MFMA(f1, asfrag(st[k]), a11); }
        }
        STAMP(5 + tt);
    }
    BARRIER();
    if (b == 0 && tid == 512) __hip_atomic_store((GAS u64*)(ws + F_EPOCH), tag, __ATOMIC_RELAXED, __HIP_MEMORY_SCOPE_AGENT);
    auto put = [&](const f32x16& a, int ti) {
        if (ti != m) {
#pragma unroll
            for (int q = 0; q < 4; ++q) *(LAS f32x4*)(lds + ((ti * 4 + m) * 4 + q) * 1024 + lo16) = (f32x4){a[4 * q], a[4 * q + 1], a[4 * q + 2], a[4 * q + 3]};
        } };
    put(a00, 0); put(a01, 1); put(a10, 2); put(a11, 3);
    BARRIER();
    f32x16 acc = m == 0 ? a00 : m == 1 ? a01 : m == 2 ? a10 : a11;
#pragma unroll
    for (int o = 1; o < 4; ++o) {
        const int src = (m + o) & 3;
#pragma unroll
        for (int q = 0; q < 4; ++q) { const f32x4 v = *(LAS const f32x4*)(lds + ((m * 4 + src) * 4 + q) * 1024 + lo16); acc[4 * q] += v[0]; acc[4 * q + 1] += v[1]; acc[4 * q + 2] += v[2]; acc[4 * q + 3] += v[3]; }
    }
    const float g = __builtin_amdgcn_rcpf(1.f + ((m & 1) ? rhoB : rhoA));
    char* op = out + (size_t)(64 * tg + (m & 1) * 32 + (ln & 31)) * 1024 + hg * 128 + (m >> 1) * 64 + (ln >> 5) * 16;
#pragma unroll
    for (int p = 0; p < 2; ++p) {
        const unsigned a0 = cvtpk(acc[8 * p] * g, acc[8 * p + 1] * g), a1 = cvtpk(acc[8 * p + 2] * g, acc[8 * p + 3] * g);
        const unsigned b0 = cvtpk(acc[8 * p + 4] * g, acc[8 * p + 5] * g), b1v = cvtpk(acc[8 * p + 6] * g, acc[8 * p + 7] * g);
        auto r0 = __builtin_amdgcn_permlane32_swap(a0, b0, false, false);
        auto r1 = __builtin_amdgcn_permlane32_swap(a1, b1v, false, false);
        *(u32x4*)(op + p * 32) = (u32x4){r0[0], r1[0], r0[1], r1[1]};
    }
    STAMP(7);
    STAMP_FLUSH();
}

__global__ void reporter(char* ws) {
    const u64 t0 = __builtin_amdgcn_s_memrealtime();
    const u64* p = (const u64*)(ws + WS_ST) + threadIdx.x * 256;
    const u64 base = ((const u64*)(ws + WS_ST))[0] - 100000ull;
    __shared__ unsigned red[2][4];
    unsigned a = ~0u, bb = 0;
    for (int w = 0; w < 12; ++w) a = min(a, (unsigned)(p[w * 16] - base));
    for (int w = W0; w < W1; ++w) bb = max(bb, (unsigned)(p[w * 16 + SI] - base));
    for (int m = 32; m >= 1; m >>= 1) { a = min(a, (unsigned)__shfl_xor((int)a, m)); bb = max(bb, (unsigned)__shfl_xor((int)bb, m)); }
    if ((threadIdx.x & 63) == 0) { red[0][threadIdx.x >> 6] = a; red[1][threadIdx.x >> 6] = bb; }
    __syncthreads();
    a = min(min(red[0][0], red[0][1]), min(red[0][2], red[0][3])); bb = max(max(red[1][0], red[1][1]), max(red[1][2], red[1][3]));
    u64 X = ZERO ? 0 : (GAP ? (unsigned)(t0 - base) - bb : bb - a);
    if (X > 20000) X = 20000;
    while (__builtin_amdgcn_s_memrealtime() < t0 + 500 + X) __builtin_amdgcn_s_sleep(1);
}
extern "C" void kernel_launch(void* const* d_in, const int* in_sizes, int n_in, void* d_out, int out_size, void* d_ws, size_t ws_size, hipStream_t stream) {
    (void)hipFuncSetAttribute((const void*)fused_kernel, hipFuncAttributeMaxDynamicSharedMemorySize, LDS_BYTES);
    fused_kernel<<<256, 768, LDS_BYTES, stream>>>((const char*)d_in[0], (const char*)d_in[1], (const char*)d_in[2], (const char*)d_in[4], (const unsigned*)d_in[5],
                                                  (const char*)d_in[6], (const char*)d_in[7], (char*)d_ws, (char*)d_out);
    reporter<<<1,256,0,stream>>>((char*)d_ws);
}
#include <vector>
#include <algorithm>
#include <cstdio>
static void dump_stamps(void* ws) {
    std::vector<u64> h(256 * 16 * 16); (void)hipMemcpy(h.data(), (char*)ws + WS_ST, h.size() * 8, hipMemcpyDeviceToHost);
    u64 t0 = ~0ull; for (int b = 0; b < 256; ++b) for (int w = 0; w < 12; ++w) t0 = std::min(t0, h[(b * 16 + w) * 16]);
    { std::vector<double> c, f; for (int b = 0; b < 256; ++b) for (int w = 0; w < 8; ++w) { const u64* p = &h[(b * 16 + w) * 16]; c.push_back((double)(p[11] - p[10])); f.push_back((double)(p[11] - p[10]) / ((p[4] - p[3]) * 10.0)); }
      std::sort(c.begin(), c.end()); std::sort(f.begin(), f.end()); printf("attn loop cycles: min %.0f med %.0f max %.0f ; clock min %.2f med %.2f max %.2f GHz\n", c[0], c[c.size()/2], c.back(), f[0], f[f.size()/2], f.back()); }
    const char* nm[3] = {"attn w0-3", "attn w4-7", "mlp w8-11"};
    for (int g = 0; g < 3; ++g) for (int i = 0; i < 16; ++i) { std::vector<double> v; for (int b = 0; b < 256; ++b) for (int w = 4 * g; w < 4 * g + 4; ++w) { u64 t = h[(b * 16 + w) * 16 + i]; if (t >= t0 && t - t0 < 100000) v.push_back((t - t0) * 0.01); }
        if (v.size() < 16) continue; std::sort(v.begin(), v.end()); printf("%s stamp %d: min %.2f med %.2f max %.2f us (n=%zu)\n", nm[g], i, v[0], v[v.size() / 2], v.back(), v.size()); }
}
```

```cpp
#include <hip/hip_runtime.h>
#include <hip/hip_bf16.h>
#include <stdint.h>
#include <type_traits>

using bf16x8 = __attribute__((ext_vector_type(8))) short;
using s16x4  = __attribute__((ext_vector_type(4))) short;
using f32x16 = __attribute__((ext_vector_type(16))) float;
using f32x4  = __attribute__((ext_vector_type(4))) float;
using f32x2  = __attribute__((ext_vector_type(2))) float;
using u32x4  = __attribute__((ext_vector_type(4))) unsigned;
using u32x2  = __attribute__((ext_vector_type(2))) unsigned;
typedef __bf16 bf16x2 __attribute__((ext_vector_type(2)));
typedef short v4i16_t __attribute__((ext_vector_type(4)));
typedef unsigned long long u64;
#define DI __device__ __forceinline__
#define LAS __attribute__((address_space(3)))
#define GAS __attribute__((address_space(1)))
#define MFMA(a, b, c) __builtin_amdgcn_mfma_f32_32x32x16_bf16((a), (b), (c), 0, 0, 0)
typedef LAS char* lds_ptr;

constexpr unsigned WS_Q = 0, WS_K = 2u << 20, WS_V = 4u << 20, WS_O = 6u << 20, WS_H = 8u << 20, WS_F = 16u << 20;
constexpr unsigned WS_BYTES = (16u << 20) + 65536;
constexpr unsigned F_QKV = WS_F, F_H = WS_F + 4096, F_O = WS_F + 16384, F_EPOCH = WS_F + 40960;
constexpr unsigned L_K = 0, L_V = 65536, L_B = 131072, L_MISC = L_B + 6144, LDS_BYTES = 163840;
#define PRIO_DROP 11
constexpr unsigned SPIN_CAP = 1u << 16;
constexpr float LOG2E = 1.4426950408889634f;
constexpr float QSCALE = 0.25f * LOG2E;

DI unsigned cvtpk(float lo, float hi) { return __builtin_bit_cast(unsigned, __builtin_convertvector((f32x2){lo, hi}, bf16x2)); }
DI u32x4 pack8(const f32x16& a, int s) { return (u32x4){cvtpk(a[8*s], a[8*s+1]), cvtpk(a[8*s+2], a[8*s+3]), cvtpk(a[8*s+4], a[8*s+5]), cvtpk(a[8*s+6], a[8*s+7])}; }
DI void glds16(const void* sbase, unsigned voff, unsigned lds_dst) {
    unsigned sv; asm volatile("s_mov_b32 %0, m0\n\ts_mov_b32 m0, %3\n\ts_nop 0\n\tglobal_load_lds_dwordx4 %1, %2\n\ts_mov_b32 m0, %0" : "=&s"(sv) : "v"(voff), "s"(sbase), "s"(lds_dst) : "memory"); }
DI void glds16_dev(const void* sbase, unsigned voff, unsigned lds_dst) {
    unsigned sv; asm volatile("s_mov_b32 %0, m0\n\ts_mov_b32 m0, %3\n\ts_nop 0\n\tglobal_load_lds_dwordx4 %1, %2 sc1\n\ts_mov_b32 m0, %0" : "=&s"(sv) : "v"(voff), "s"(sbase), "s"(lds_dst) : "memory"); }
DI void GLD(u32x4& dst, const void* sbase, unsigned voff) { asm volatile("global_load_dwordx4 %0, %1, %2" : "=v"(dst) : "v"(voff), "s"(sbase) : "memory"); }
DI void GLD_DEV(u32x4& dst, const void* sbase, unsigned voff) { asm volatile("global_load_dwordx4 %0, %1, %2 sc1" : "=v"(dst) : "v"(voff), "s"(sbase) : "memory"); }
DI void gst_dev(const void* sbase, unsigned voff, u32x4 v) { asm volatile("global_store_dwordx4 %0, %1, %2 sc1\n\ts_nop 1" :: "v"(voff), "v"(v), "s"(sbase) : "memory"); }
DI s16x4 trd(lds_ptr p) { return __builtin_bit_cast(s16x4, __builtin_amdgcn_ds_read_tr16_b64_v4i16((LAS v4i16_t*)p)); }
DI bf16x8 trfrag(lds_ptr p, int second) { s16x4 a = trd(p), b = trd(p + second); return __builtin_shufflevector(a, b, 0, 1, 2, 3, 4, 5, 6, 7); }
DI bf16x8 ldfrag(lds_ptr p) { return *(LAS const bf16x8*)p; }
DI bf16x8 asfrag(u32x4 v) { return __builtin_bit_cast(bf16x8, v); }
#define PACE_N 1
#define PACE() do { __builtin_amdgcn_sched_barrier(0); if (PACE_N) __builtin_amdgcn_s_sleep(PACE_N); __builtin_amdgcn_sched_barrier(0); } while (0)
#define WAITV(n) asm volatile("s_waitcnt vmcnt(%0)" :: "i"(n) : "memory")
#define BARRIER() do { asm volatile("" ::: "memory"); __builtin_amdgcn_s_waitcnt(0xC07F); __builtin_amdgcn_s_barrier(); asm volatile("" ::: "memory"); } while (0)
DI float mish(float h) {
    const float u = __builtin_amdgcn_exp2f(__builtin_fminf(h * LOG2E, 40.f));
    const float n = u * (u + 2.f);
    return h * n * __builtin_amdgcn_rcpf(n + 2.f);
}
DI float halfmax(float m) { auto rr = __builtin_amdgcn_permlane32_swap(__float_as_uint(m), __float_as_uint(m), false, false); return __builtin_fmaxf(__uint_as_float(rr[0]), __uint_as_float(rr[1])); }
DI float max16(const f32x16& p) {
    float a = __builtin_fmaxf(__builtin_fmaxf(p[0], p[1]), p[2]), b = __builtin_fmaxf(__builtin_fmaxf(p[3], p[4]), p[5]);
    a = __builtin_fmaxf(__builtin_fmaxf(a, p[6]), p[7]); b = __builtin_fmaxf(__builtin_fmaxf(b, p[8]), p[9]);
    a = __builtin_fmaxf(__builtin_fmaxf(a, p[10]), p[11]); b = __builtin_fmaxf(__builtin_fmaxf(b, p[12]), p[13]);
    a = __builtin_fmaxf(__builtin_fmaxf(a, p[14]), p[15]); return __builtin_fmaxf(a, b); }
DI void publish(char* ws, unsigned off, u64 tag, int lane) {
    asm volatile("s_waitcnt vmcnt(0)" ::: "memory");
    if (lane == 0) __hip_atomic_store((GAS u64*)(ws + off), tag, __ATOMIC_RELAXED, __HIP_MEMORY_SCOPE_AGENT);
}
DI void poll_tags(char* ws, unsigned off, int n, u64 tag, int lane) {
    GAS u64* p = (GAS u64*)(ws + off) + (lane < n ? lane : 0);
#pragma unroll 1
    for (unsigned s = 0; s < SPIN_CAP; ++s) {
        const u64 v = __hip_atomic_load(p, __ATOMIC_RELAXED, __HIP_MEMORY_SCOPE_AGENT);
        if (__all(v == tag)) break;
        __builtin_amdgcn_s_sleep(2);
    }
    asm volatile("" ::: "memory");
}
DI void lds_arrive(lds_ptr c, int lane) {
    asm volatile("s_waitcnt lgkmcnt(0)" ::: "memory");
    if (lane == 0) __hip_atomic_fetch_add((LAS unsigned*)c, 1u, __ATOMIC_RELAXED, __HIP_MEMORY_SCOPE_WORKGROUP);
}
DI void lds_wait(lds_ptr c, unsigned n) {
#pragma unroll 1
    for (unsigned s = 0; s < SPIN_CAP * 8; ++s) {
        const unsigned v = __builtin_amdgcn_readfirstlane(__hip_atomic_load((LAS unsigned*)c, __ATOMIC_RELAXED, __HIP_MEMORY_SCOPE_WORKGROUP));
        if (v >= n) break;
        __builtin_amdgcn_s_sleep(1);
    }
    asm volatile("" ::: "memory");
}
#define STAMP(i)
#define CSTAMP(i)
#define STAMP_DECL()
#define STAMP_FLUSH()
DI unsigned ximg(int kc) { return 32768u + (kc & 3) * 8192u + (kc >> 2) * 65536u; }
constexpr unsigned wimg(int n) { return n < 4 ? L_B + n * 8192u : n < 8 ? L_K + (n - 4) * 8192u : L_V + (n - 8) * 8192u; }

__global__ __launch_bounds__(768, 3) void fused_kernel(const char* __restrict__ x, const char* __restrict__ wqkv, const char* __restrict__ wo,
                                                       const char* __restrict__ w1, const unsigned* __restrict__ b1, const char* __restrict__ w2,
                                                       const char* __restrict__ gw, char* __restrict__ ws, char* __restrict__ out) {
    extern __shared__ __attribute__((aligned(16))) char lds_raw[];
    lds_ptr lds = (lds_ptr)lds_raw;
    const unsigned l0 = (unsigned)(uintptr_t)lds_raw;
    const int tid = threadIdx.x, lane = tid & 63, w = __builtin_amdgcn_readfirstlane(tid >> 6), hi = lane >> 5;
    const int b = blockIdx.x, xcd = b & 7, bi = b >> 3;
    const int tg = (xcd >> 1) * 8 + (bi & 7), hg = (xcd & 1) * 4 + (bi >> 3);
    const int rot = ((bi & 7) + 2 * (bi >> 3)) & 7;
    const unsigned lo16 = lane * 16;
    const unsigned xfo = (lane & 31) * 128 + ((hi ^ ((lane >> 1) & 7)) * 16);
    STAMP_DECL();
    STAMP(0);
    if (w < 8) {
        const int ct = (w < 4) ? (w & 1) : ((w >> 1) & 1);
        const unsigned wfo = (8 * hi + ((lane & 15) >> 2)) * 128 + ((ct ^ ((lane >> 3) & 1)) * 64) + ((lane >> 4) & 1) * 32 + (lane & 3) * 8;
        f32x16 acc0 = {}, acc1 = {};
        bf16x8 WF[2][4], XA[2][4], XC[2][4];
        if (w < 4) {
            const unsigned sgo = (w >> 1) * 8192u;
#pragma unroll
            for (int i = 0; i <= 8; ++i) {
                unsigned xb = 0, wb = 0;
                if (i < 8) {
                    BARRIER();
                    if (i == 0 || i == 7) STAMP(8 + i);
                    xb = ximg((i + rot) & 7) + xfo;
                    wb = ((i & 3) == 1 ? (w < 2 ? wimg(3) : wimg(4)) : wimg((i & 3) * 3) + sgo) + wfo;
                }
#pragma unroll
                for (int ks = 0; ks < 4; ++ks) {
                    if (i > 0) acc0 = MFMA(WF[(i - 1) & 1][ks], XA[(i - 1) & 1][ks], acc0);
                    if (i < 8) { WF[i & 1][ks] = trfrag(lds + wb + ks * 2048, 512); XA[i & 1][ks] = ldfrag(lds + (xb ^ (ks * 32))); }
                    __builtin_amdgcn_sched_barrier(0);
                    if (i > 0) acc1 = MFMA(WF[(i - 1) & 1][ks], XC[(i - 1) & 1][ks], acc1);
                    if (i < 8) XC[i & 1][ks] = ldfrag(lds + (xb ^ (ks * 32)) + 4096);
                    __builtin_amdgcn_sched_barrier(0);
                }
            }
            if (w < 2) {
#pragma unroll
                for (int i = 0; i < 16; ++i) { acc0[i] *= QSCALE; acc1[i] *= QSCALE; }
            }
            const char* dst = ws + (w < 2 ? WS_Q : WS_K) + (size_t)((4 * hg + 2 * ct) * 64 + 2 * tg) * 1024;
            gst_dev(dst, lo16, pack8(acc0, 0)); gst_dev(dst + 65536, lo16, pack8(acc0, 1));
            gst_dev(dst + 1024, lo16, pack8(acc1, 0)); gst_dev(dst + 65536 + 1024, lo16, pack8(acc1, 1));
        } else {
            const int tt = w & 1;
#pragma unroll
            for (int i = 0; i <= 8; ++i) {
                unsigned xb = 0;
                if (i < 8) {
                    BARRIER();
                    if (i == 0 || i == 7) STAMP(8 + i);
                    xb = ximg((i + rot) & 7) + xfo + tt * 4096;
                }
#pragma unroll
                for (int ks = 0; ks < 4; ++ks) {
                    if (i > 0) acc0 = MFMA(XA[(i - 1) & 1][ks], WF[(i - 1) & 1][ks], acc0);
                    if (i < 8) { WF[i & 1][ks] = trfrag(lds + wimg((i & 3) * 3 + 2) + wfo + ks * 2048, 512); XA[i & 1][ks] = ldfrag(lds + (xb ^ (ks * 32))); }
                    __builtin_amdgcn_sched_barrier(0);
                }
            }
            const char* dst = ws + WS_V + (size_t)((4 * hg + 2 * ct) * 64 + 2 * tg + tt) * 1024;
            const unsigned vo = ((lane >> 4) & 1) * 65536 + hi * 256 + (lane & 15) * 16;
            gst_dev(dst, vo, pack8(acc0, 0)); gst_dev(dst + 512, vo, pack8(acc0, 1));
        }
        STAMP(1);
        WAITV(0); BARRIER();
        STAMP(6);
        const u64 tag = *(LAS const u64*)(lds + L_MISC + 16);
        const int head = xcd * 4 + (bi & 3), r8 = bi >> 2;
        const int tile = 8 * r8 + (w < 4 ? 2 * w : 2 * (w - 4) + 1);
        if (w == 0) {
            if (lane == 0) __hip_atomic_store((GAS u64*)(ws + F_QKV + (hg * 32 + tg) * 8), tag, __ATOMIC_RELAXED, __HIP_MEMORY_SCOPE_AGENT);
            poll_tags(ws, F_QKV + (head >> 2) * 256, 32, tag, lane);
            if (lane == 0) __hip_atomic_store((LAS unsigned*)(lds + L_MISC + 24), 1u, __ATOMIC_RELAXED, __HIP_MEMORY_SCOPE_WORKGROUP);
        } else lds_wait(lds + L_MISC + 24, 1);
        STAMP(2);
        u32x4 qr;
        GLD_DEV(qr, ws + WS_Q + (size_t)(head * 64 + tile) * 1024, lo16);
        const char* kb = ws + WS_K + (size_t)head * 65536; const char* vb = ws + WS_V + (size_t)head * 65536;
#pragma unroll
        for (int j = 0; j < 4; ++j) { glds16_dev(kb + (j * 8 + w) * 1024, lo16, l0 + L_K + (j * 8 + w) * 1024); glds16_dev(vb + (j * 8 + w) * 1024, lo16, l0 + L_V + (j * 8 + w) * 1024); }
        asm volatile("s_waitcnt vmcnt(6)" : "+v"(qr) :: "memory");
        const bf16x8 qf = asfrag(qr);
        lds_arrive(lds + L_MISC, lane); lds_wait(lds + L_MISC, 8);
        STAMP(3); CSTAMP(10);
        if (w >= 4) __builtin_amdgcn_s_setprio(1);
        const f32x16 zero = {};
        const float nm = -halfmax(max16(MFMA(ldfrag(lds + L_K + lo16), qf, zero)));
        float o[9];
        {
            unsigned ka = l0 + L_K + lo16, va = l0 + L_V + hi * 256 + (lane & 15) * 16; const unsigned cnt_addr = l0 + L_MISC;
            unsigned s_it, s_sp, s_st;
            const unsigned onev = ((lane & 27) == 16) ? 0x3f803f80u : 0u;
    asm volatile(
        "ds_read_b128 v[136:139], %[ka]\n\t"
        "ds_read_b128 v[140:143], %[ka] offset:1024\n\t"
        "v_mov_b32 v144, %[one]\n\t"
        "v_mov_b32 v148, %[one]\n\t"
        "v_mov_b32 v145, %[one]\n\t"
        "v_mov_b32 v149, %[one]\n\t"
        "v_mov_b32 v146, %[one]\n\t"
        "v_mov_b32 v150, %[one]\n\t"
        "v_mov_b32 v147, %[one]\n\t"
        "v_mov_b32 v151, %[one]\n\t"
        "v_mov_b32 v152, %[one]\n\t"
        "v_mov_b32 v156, %[one]\n\t"
        "v_mov_b32 v153, %[one]\n\t"
        "v_mov_b32 v157, %[one]\n\t"
        "v_mov_b32 v154, %[one]\n\t"
        "v_mov_b32 v158, %[one]\n\t"
        "v_mov_b32 v155, %[one]\n\t"
        "v_mov_b32 v159, %[one]\n\t"
        "s_mov_b64 exec, %[vm]\n\t"
        "ds_read_b128 v[144:147], %[va] offset:0\n\t"
        "ds_read_b128 v[148:151], %[va] offset:512\n\t"
        "s_mov_b64 exec, -1\n\t"
        "v_mov_b32 v112, %[nm]\n\t"
        "v_mov_b32 v113, %[nm]\n\t"
        "v_mov_b32 v114, %[nm]\n\t"
        "v_mov_b32 v115, %[nm]\n\t"
        "v_mov_b32 v116, %[nm]\n\t"
        "v_mov_b32 v117, %[nm]\n\t"
        "v_mov_b32 v118, %[nm]\n\t"
        "v_mov_b32 v119, %[nm]\n\t"
        "v_mov_b32 v120, %[nm]\n\t"
        "v_mov_b32 v121, %[nm]\n\t"
        "v_mov_b32 v122, %[nm]\n\t"
        "v_mov_b32 v123, %[nm]\n\t"
        "v_mov_b32 v124, %[nm]\n\t"
        "v_mov_b32 v125, %[nm]\n\t"
        "v_mov_b32 v126, %[nm]\n\t"
        "v_mov_b32 v127, %[nm]\n\t"
        "v_mov_b32 v96, 0\n\t"
        "v_mov_b32 v97, 0\n\t"
        "v_mov_b32 v98, 0\n\t"
        "v_mov_b32 v99, 0\n\t"
        "v_mov_b32 v100, 0\n\t"
        "v_mov_b32 v101, 0\n\t"
        "v_mov_b32 v102, 0\n\t"
        "v_mov_b32 v103, 0\n\t"
        "v_mov_b32 v104, 0\n\t"
        "v_mov_b32 v105, 0\n\t"
        "v_mov_b32 v106, 0\n\t"
        "v_mov_b32 v107, 0\n\t"
        "v_mov_b32 v108, 0\n\t"
        "v_mov_b32 v109, 0\n\t"
        "v_mov_b32 v110, 0\n\t"
        "v_mov_b32 v111, 0\n\t"
        "v_mov_b32 v132, 0\n\t"
        "v_mov_b32 v133, 0\n\t"
        "v_mov_b32 v134, 0\n\t"
        "v_mov_b32 v135, 0\n\t"
        "s_mov_b32 %[it], 0\n\t"
        "s_waitcnt lgkmcnt(3)\n\t"
        "v_mfma_f32_32x32x16_bf16 v[64:79], v[136:139], %[q], v[112:127]\n\t"
        "1:\n\t"
        "s_cmp_eq_u32 %[it], 1\n\t"
        "s_cbranch_scc1 3f\n\t"
        "s_cmp_eq_u32 %[it], 7\n\t"
        "s_cbranch_scc1 4f\n\t"
        "s_cmp_lg_u32 %[it], %[pd]\n\t"
        "s_cbranch_scc1 2f\n\t"
        "s_setprio 0\n\t"
        "2:\n\t"
        "s_waitcnt lgkmcnt(2)\n\t"
        "v_mfma_f32_32x32x16_bf16 v[80:95], v[140:143], %[q], v[112:127]\n\t"
        "ds_read_b128 v[136:139], %[ka] offset:2048\n\t"
        "v_exp_f32 v64, v64\n\t"
        "v_exp_f32 v65, v65\n\t"
        "v_exp_f32 v66, v66\n\t"
        "v_exp_f32 v67, v67\n\t"
        "v_exp_f32 v68, v68\n\t"
        "v_exp_f32 v69, v69\n\t"
        "v_mfma_f32_32x32x16_bf16 v[96:111], v[156:159], v[132:135], v[96:111]\n\t"
        "s_mov_b64 exec, %[vm]\n\t"
        "ds_read_b128 v[152:155], %[va] offset:1024\n\t"
        "ds_read_b128 v[156:159], %[va] offset:1536\n\t"
        "s_mov_b64 exec, -1\n\t"
        "v_exp_f32 v70, v70\n\t"
        "v_exp_f32 v71, v71\n\t"
        "v_cvt_pk_bf16_f32 v128, v64, v65\n\t"
        "v_cvt_pk_bf16_f32 v129, v66, v67\n\t"
        "v_cvt_pk_bf16_f32 v130, v68, v69\n\t"
        "v_cvt_pk_bf16_f32 v131, v70, v71\n\t"
        "v_exp_f32 v72, v72\n\t"
        "v_exp_f32 v73, v73\n\t"
        "v_exp_f32 v74, v74\n\t"
        "s_waitcnt lgkmcnt(3)\n\t"
        "v_mfma_f32_32x32x16_bf16 v[96:111], v[144:147], v[128:131], v[96:111]\n\t"
        "v_exp_f32 v75, v75\n\t"
        "v_exp_f32 v76, v76\n\t"
        "v_exp_f32 v77, v77\n\t"
        "v_exp_f32 v78, v78\n\t"
        "v_exp_f32 v79, v79\n\t"
        "v_cvt_pk_bf16_f32 v132, v72, v73\n\t"
        "v_cvt_pk_bf16_f32 v133, v74, v75\n\t"
        "v_cvt_pk_bf16_f32 v134, v76, v77\n\t"
        "v_cvt_pk_bf16_f32 v135, v78, v79\n\t"
        "s_waitcnt lgkmcnt(2)\n\t"
        "v_mfma_f32_32x32x16_bf16 v[64:79], v[136:139], %[q], v[112:127]\n\t"
        "ds_read_b128 v[140:143], %[ka] offset:3072\n\t"
        "v_exp_f32 v80, v80\n\t"
        "v_exp_f32 v81, v81\n\t"
        "v_exp_f32 v82, v82\n\t"
        "v_exp_f32 v83, v83\n\t"
        "v_exp_f32 v84, v84\n\t"
        "v_exp_f32 v85, v85\n\t"
        "v_mfma_f32_32x32x16_bf16 v[96:111], v[148:151], v[132:135], v[96:111]\n\t"
        "s_mov_b64 exec, %[vm]\n\t"
        "ds_read_b128 v[144:147], %[va] offset:2048\n\t"
        "ds_read_b128 v[148:151], %[va] offset:2560\n\t"
        "s_mov_b64 exec, -1\n\t"
        "v_exp_f32 v86, v86\n\t"
        "v_exp_f32 v87, v87\n\t"
        "v_cvt_pk_bf16_f32 v128, v80, v81\n\t"
        "v_cvt_pk_bf16_f32 v129, v82, v83\n\t"
        "v_cvt_pk_bf16_f32 v130, v84, v85\n\t"
        "v_cvt_pk_bf16_f32 v131, v86, v87\n\t"
        "v_exp_f32 v88, v88\n\t"
        "v_exp_f32 v89, v89\n\t"
        "v_exp_f32 v90, v90\n\t"
        "s_waitcnt lgkmcnt(3)\n\t"
        "v_mfma_f32_32x32x16_bf16 v[96:111], v[152:155], v[128:131], v[96:111]\n\t"
        "v_exp_f32 v91, v91\n\t"
        "v_exp_f32 v92, v92\n\t"
        "v_exp_f32 v93, v93\n\t"
        "v_exp_f32 v94, v94\n\t"
        "v_exp_f32 v95, v95\n\t"
        "v_cvt_pk_bf16_f32 v132, v88, v89\n\t"
        "v_cvt_pk_bf16_f32 v133, v90, v91\n\t"
        "v_cvt_pk_bf16_f32 v134, v92, v93\n\t"
        "v_cvt_pk_bf16_f32 v135, v94, v95\n\t"
        "s_waitcnt lgkmcnt(2)\n\t"
        "v_mfma_f32_32x32x16_bf16 v[80:95], v[140:143], %[q], v[112:127]\n\t"
        "ds_read_b128 v[136:139], %[ka] offset:4096\n\t"
        "v_exp_f32 v64, v64\n\t"
        "v_exp_f32 v65, v65\n\t"
        "v_exp_f32 v66, v66\n\t"
        "v_exp_f32 v67, v67\n\t"
        "v_exp_f32 v68, v68\n\t"
        "v_exp_f32 v69, v69\n\t"
        "v_mfma_f32_32x32x16_bf16 v[96:111], v[156:159], v[132:135], v[96:111]\n\t"
        "s_mov_b64 exec, %[vm]\n\t"
        "ds_read_b128 v[152:155], %[va] offset:3072\n\t"
        "ds_read_b128 v[156:159], %[va] offset:3584\n\t"
        "s_mov_b64 exec, -1\n\t"
        "v_exp_f32 v70, v70\n\t"
        "v_exp_f32 v71, v71\n\t"
        "v_cvt_pk_bf16_f32 v128, v64, v65\n\t"
        "v_cvt_pk_bf16_f32 v129, v66, v67\n\t"
        "v_cvt_pk_bf16_f32 v130, v68, v69\n\t"
        "v_cvt_pk_bf16_f32 v131, v70, v71\n\t"
        "v_exp_f32 v72, v72\n\t"
        "v_exp_f32 v73, v73\n\t"
        "v_exp_f32 v74, v74\n\t"
        "s_waitcnt lgkmcnt(3)\n\t"
        "v_mfma_f32_32x32x16_bf16 v[96:111], v[144:147], v[128:131], v[96:111]\n\t"
        "v_exp_f32 v75, v75\n\t"
        "v_exp_f32 v76, v76\n\t"
        "v_exp_f32 v77, v77\n\t"
        "v_exp_f32 v78, v78\n\t"
        "v_exp_f32 v79, v79\n\t"
        "v_cvt_pk_bf16_f32 v132, v72, v73\n\t"
        "v_cvt_pk_bf16_f32 v133, v74, v75\n\t"
        "v_cvt_pk_bf16_f32 v134, v76, v77\n\t"
        "v_cvt_pk_bf16_f32 v135, v78, v79\n\t"
        "s_waitcnt lgkmcnt(2)\n\t"
        "v_mfma_f32_32x32x16_bf16 v[64:79], v[136:139], %[q], v[112:127]\n\t"
        "ds_read_b128 v[140:143], %[ka] offset:5120\n\t"
        "v_exp_f32 v80, v80\n\t"
        "v_exp_f32 v81, v81\n\t"
        "v_exp_f32 v82, v82\n\t"
        "v_exp_f32 v83, v83\n\t"
        "v_exp_f32 v84, v84\n\t"
        "v_exp_f32 v85, v85\n\t"
        "v_mfma_f32_32x32x16_bf16 v[96:111], v[148:151], v[132:135], v[96:111]\n\t"
        "s_mov_b64 exec, %[vm]\n\t"
        "ds_read_b128 v[144:147], %[va] offset:4096\n\t"
        "ds_read_b128 v[148:151], %[va] offset:4608\n\t"
        "s_mov_b64 exec, -1\n\t"
        "v_exp_f32 v86, v86\n\t"
        "v_exp_f32 v87, v87\n\t"
        "v_cvt_pk_bf16_f32 v128, v80, v81\n\t"
        "v_cvt_pk_bf16_f32 v129, v82, v83\n\t"
        "v_cvt_pk_bf16_f32 v130, v84, v85\n\t"
        "v_cvt_pk_bf16_f32 v131, v86, v87\n\t"
        "v_exp_f32 v88, v88\n\t"
        "v_exp_f32 v89, v89\n\t"
        "v_exp_f32 v90, v90\n\t"
        "s_waitcnt lgkmcnt(3)\n\t"
        "v_mfma_f32_32x32x16_bf16 v[96:111], v[152:155], v[128:131], v[96:111]\n\t"
        "v_exp_f32 v91, v91\n\t"
        "v_exp_f32 v92, v92\n\t"
        "v_exp_f32 v93, v93\n\t"
        "v_exp_f32 v94, v94\n\t"
        "v_exp_f32 v95, v95\n\t"
        "v_add_u32 %[ka], 0x1000, %[ka]\n\t"
        "v_add_u32 %[va], 0x1000, %[va]\n\t"
        "v_cvt_pk_bf16_f32 v132, v88, v89\n\t"
        "v_cvt_pk_bf16_f32 v133, v90, v91\n\t"
        "v_cvt_pk_bf16_f32 v134, v92, v93\n\t"
        "v_cvt_pk_bf16_f32 v135, v94, v95\n\t"
        "s_add_u32 %[it], %[it], 1\n\t"
        "s_cmp_lt_u32 %[it], 16\n\t"
        "s_cbranch_scc1 1b\n\t"
        "s_branch 9f\n\t"
        "3:\n\t"
        "s_waitcnt vmcnt(0)\n\t"
        "v_mov_b32 v160, 1\n\t"
        "s_mov_b64 exec, 1\n\t"
        "ds_add_u32 %[cn], v160 offset:4\n\t"
        "s_mov_b64 exec, -1\n\t"
        "s_mov_b32 %[sp], 0\n\t"
        "5:\n\t"
        "ds_read_b32 v160, %[cn] offset:4\n\t"
        "s_waitcnt lgkmcnt(0)\n\t"
        "v_readfirstlane_b32 %[st], v160\n\t"
        "s_add_u32 %[sp], %[sp], 1\n\t"
        "s_cmp_ge_u32 %[st], 8\n\t"
        "s_cbranch_scc1 2b\n\t"
        "s_sleep 1\n\t"
        "s_cmp_lt_u32 %[sp], %[cap]\n\t"
        "s_cbranch_scc1 5b\n\t"
        "s_branch 2b\n\t"
        "4:\n\t"
        "s_mov_b32 %[sp], 0\n\t"
        "6:\n\t"
        "ds_read_b32 v160, %[cn] offset:8\n\t"
        "s_waitcnt lgkmcnt(0)\n\t"
        "v_readfirstlane_b32 %[st], v160\n\t"
        "s_add_u32 %[sp], %[sp], 1\n\t"
        "s_cmp_ge_u32 %[st], 4\n\t"
        "s_cbranch_scc1 2b\n\t"
        "s_sleep 2\n\t"
        "s_cmp_lt_u32 %[sp], %[cap]\n\t"
        "s_cbranch_scc1 6b\n\t"
        "s_branch 2b\n\t"
        "9:\n\t"
        "s_nop 1\n\t"
        "v_mfma_f32_32x32x16_bf16 v[96:111], v[156:159], v[132:135], v[96:111]\n\t"
        "s_waitcnt lgkmcnt(0)\n\t"
        "s_nop 15\n\t"
        "v_mov_b32 %[o0], v96\n\t"
        "v_mov_b32 %[o1], v97\n\t"
        "v_mov_b32 %[o2], v98\n\t"
        "v_mov_b32 %[o3], v99\n\t"
        "v_mov_b32 %[o4], v100\n\t"
        "v_mov_b32 %[o5], v101\n\t"
        "v_mov_b32 %[o6], v102\n\t"
        "v_mov_b32 %[o7], v103\n\t"
        "v_mov_b32 %[o8], v104\n\t"
        : [o0] "=&v"(o[0]), [o1] "=&v"(o[1]), [o2] "=&v"(o[2]), [o3] "=&v"(o[3]), [o4] "=&v"(o[4]), [o5] "=&v"(o[5]), [o6] "=&v"(o[6]), [o7] "=&v"(o[7]), [o8] "=&v"(o[8]), [ka] "+v"(ka), [va] "+v"(va), [it] "=&s"(s_it), [sp] "=&s"(s_sp), [st] "=&s"(s_st)
        : [q] "v"(qf), [nm] "v"(nm), [one] "v"(onev), [vm] "s"(0x0000ffff0000ffffull), [cn] "v"(cnt_addr), [cap] "s"(SPIN_CAP), [pd] "i"(PRIO_DROP)
        : "memory", "scc", "v64", "v65", "v66", "v67", "v68", "v69", "v70", "v71", "v72", "v73", "v74", "v75", "v76", "v77", "v78", "v79", "v80", "v81", "v82", "v83", "v84", "v85", "v86", "v87", "v88", "v89", "v90", "v91", "v92", "v93", "v94", "v95", "v96", "v97", "v98", "v99", "v100", "v101", "v102", "v103", "v104", "v105", "v106", "v107", "v108", "v109", "v110", "v111", "v112", "v113", "v114", "v115", "v116", "v117", "v118", "v119", "v120", "v121", "v122", "v123", "v124", "v125", "v126", "v127", "v128", "v129", "v130", "v131", "v132", "v133", "v134", "v135", "v136", "v137", "v138", "v139", "v140", "v141", "v142", "v143", "v144", "v145", "v146", "v147", "v148", "v149", "v150", "v151", "v152", "v153", "v154", "v155", "v156", "v157", "v158", "v159", "v160");
        }
        STAMP(4); CSTAMP(11);
        float big = __builtin_fabsf(o[8]);
#pragma unroll
        for (int i = 0; i < 8; ++i) big = __builtin_fmaxf(big, __builtin_fabsf(o[i]) * 0x1p-60f);
        if (__builtin_expect(__any(!(big < 0x1p60f) || !(o[8] > 0x1p-20f)), 0)) {
            float mr = -INFINITY; f32x16 oo = zero;
            const bf16x8 ones = asfrag((u32x4){0x3f803f80u, 0x3f803f80u, 0x3f803f80u, 0x3f803f80u});
            for (int t = 0; t < 64; ++t) {
                f32x16 s = MFMA(ldfrag(lds + L_K + t * 1024 + lo16), qf, zero);
                const float mn = __builtin_fmaxf(mr, halfmax(max16(s)));
                const float al = __builtin_amdgcn_exp2f(mr - mn); mr = mn;
#pragma unroll
                for (int i = 0; i < 16; ++i) { oo[i] *= al; s[i] = __builtin_amdgcn_exp2f(s[i] - mn); }
                lds_ptr vp = lds + L_V + t * 1024 + hi * 256 + (lane & 15) * 16;
                const bf16x8 v0 = ldfrag(vp), v1 = ldfrag(vp + 512);
                oo = MFMA((lane & 16) ? ones : v0, asfrag(pack8(s, 0)), oo);
                oo = MFMA((lane & 16) ? ones : v1, asfrag(pack8(s, 1)), oo);
            }
#pragma unroll
            for (int i = 0; i < 9; ++i) o[i] = oo[i];
        }
        const float rl = __builtin_amdgcn_rcpf(o[8]);
        gst_dev(ws + WS_O + (size_t)(tile * 32 + head) * 1024, lo16,
                (u32x4){cvtpk(o[0] * rl, o[1] * rl), cvtpk(o[2] * rl, o[3] * rl), cvtpk(o[4] * rl, o[5] * rl), cvtpk(o[6] * rl, o[7] * rl)});
        publish(ws, F_O + (tile * 32 + head) * 8, tag, lane);
        STAMP(5);
        STAMP_FLUSH();
        __builtin_amdgcn_endpgm();
    }
    const int m = w - 8;
    lds_ptr ring = lds + L_B + m * 8192;
    const unsigned stw = (lane >> 3) * 128 + (((lane & 7) ^ (((lane >> 4) & 1) << 2)) * 16);
    const unsigned trb = ((lane & 15) >> 2) * 128 + (((lane >> 3) & 1) * 64) + ((lane >> 4) & 1) * 32 + (lane & 3) * 8;
    const int cp = (hg >> 1) * 14 + (hg & 1) * 8 + 2 * m;
    const bool hact = m < ((hg & 1) ? 3 : 4);
    const char* w1s = w1 + (hact ? cp : 0) * 64;
    const unsigned w1vo = (lane >> 3) * 3584 + (lane & 7) * 16;
    u32x4 st[16];
    u64 tag;
    float zA, zB;
    f32x16 a00, a01, a10, a11;
    {
        const char* xs = x + (size_t)(64 * tg + 16 * m) * 1024;
        const unsigned xvo = (lane >> 3) * 1024 + (((lane & 7) ^ (lane >> 4)) * 16);
        const char* wsrc = wqkv + (size_t)(16 * m) * 3072 + hg * 128;
        const unsigned wvo = (lane >> 3) * 3072 + (((lane & 7) ^ (((lane >> 4) & 1) << 2)) * 16);
        auto issue = [&](int i) { const int kc = (i + rot) & 7;
            glds16(xs + kc * 128, xvo, l0 + ximg(kc) + m * 2048); glds16(xs + kc * 128 + 8192, xvo ^ 64, l0 + ximg(kc) + m * 2048 + 1024);
#pragma unroll
            for (int sg = 0; sg < 3; ++sg) { const char* p = wsrc + (size_t)kc * 64 * 3072 + sg * 1024; const unsigned d = l0 + wimg((i & 3) * 3 + sg) + m * 2048;
                glds16(p, wvo, d); glds16(p + 8 * 3072, wvo, d + 1024); } };
        auto w1load = [&](int k) { const int kk = (k + 4 * rot) & 31; GLD(st[2 * k], w1s + (size_t)(16 * kk) * 3584, w1vo); GLD(st[2 * k + 1], w1s + (size_t)(16 * kk + 8) * 3584, w1vo); };
        issue(0); issue(1);
        u64 ep; u32x2 bv[8];
        constexpr int WCNT[8] = {8, 8, 17, 17, 8, 8, 8, 7};
#pragma unroll
        for (int i = 0; i < 8; ++i) {
            if (i == 4) asm volatile("s_waitcnt vmcnt(8)" : "+v"(ep), "+v"(bv[0]), "+v"(bv[1]), "+v"(bv[2]), "+v"(bv[3]), "+v"(bv[4]), "+v"(bv[5]), "+v"(bv[6]), "+v"(bv[7]) :: "memory");
            else WAITV(WCNT[i]);
            BARRIER();
            if (i <= 5) issue(i + 2);
            if (i == 1) {
                asm volatile("global_load_dwordx2 %0, %1, %2 sc1" : "=v"(ep) : "v"(0u), "s"(ws + F_EPOCH) : "memory");
#pragma unroll
                for (int g = 0; g < 8; ++g) asm volatile("global_load_dwordx2 %0, %1, %2" : "=v"(bv[g]) : "v"(hi * 8), "s"(b1 + (hact ? cp : 0) * 16 + 4 * g) : "memory");
            }
            if (i == 6) { glds16(gw, lo16, l0 + L_B + m * 8192 + 7168); w1load(0); w1load(1); w1load(2); }
            if (i == 7) { w1load(3); w1load(4); }
        }
#pragma unroll
        for (int g = 0; g < 4; ++g)
#pragma unroll
            for (int j = 0; j < 4; ++j) {
                const unsigned u0 = bv[g][j >> 1], u1 = bv[4 + g][j >> 1];
                a00[4 * g + j] = __uint_as_float((j & 1) ? (u0 & 0xffff0000u) : (u0 << 16));
                a10[4 * g + j] = __uint_as_float((j & 1) ? (u1 & 0xffff0000u) : (u1 << 16));
            }
        if (m == 0 && lane == 0) { *(LAS u32x4*)(lds + L_MISC) = (u32x4){0u, 0u, 0u, 0u}; *(LAS u64*)(lds + L_MISC + 16) = ep + 1; *(LAS u64*)(lds + L_MISC + 24) = 0; }
        BARRIER();
        tag = *(LAS const u64*)(lds + L_MISC + 16);
    }
    {
        a01 = a00; a11 = a10;
        const unsigned trn = trb + 8 * hi * 128;
        __builtin_amdgcn_s_setprio(2);
        constexpr int HD = 5;
        bf16x8 f0[2], f1[2], xa[2], xc[2];
        float pA = 0.f, pB = 0.f;
        u32x4 gv;
#pragma unroll
        for (int k = 0; k <= 32; ++k) {
            const int r = k % HD;
            lds_ptr sl = ring + (k & 1) * 2048;
            unsigned xb = 0;
            if (k < 32) {
                asm volatile("s_waitcnt vmcnt(%2)" : "+v"(st[2 * r]), "+v"(st[2 * r + 1]) : "i"(k + HD <= 32 ? 2 * HD - 2 : 2 * (31 - k)) : "memory");
                *(LAS u32x4*)(sl + stw) = st[2 * r]; *(LAS u32x4*)(sl + 1024 + stw) = st[2 * r + 1];
                xb = (ximg(((k >> 2) + rot) & 7) + xfo) ^ ((k & 3) * 32);
            }
            if (k > 0) a00 = MFMA(f0[(k - 1) & 1], xa[(k - 1) & 1], a00);
            if (k < 32) { xa[k & 1] = ldfrag(lds + xb); f0[k & 1] = trfrag(sl + trn, 512); }
            __builtin_amdgcn_sched_barrier(0);
            if (k > 0) a10 = MFMA(f1[(k - 1) & 1], xa[(k - 1) & 1], a10);
            if (k < 32) { f1[k & 1] = trfrag(sl + (trn ^ 64), 512); xc[k & 1] = ldfrag(lds + xb + 4096); }
            __builtin_amdgcn_sched_barrier(0);
            if (k > 0) a01 = MFMA(f0[(k - 1) & 1], xc[(k - 1) & 1], a01);
            if (k + HD < 32) { const int kk = (k + HD + 4 * rot) & 31; GLD(st[2 * r], w1s + (size_t)(16 * kk) * 3584, w1vo); GLD(st[2 * r + 1], w1s + (size_t)(16 * kk + 8) * 3584, w1vo); }
            if (k > 0) a11 = MFMA(f1[(k - 1) & 1], xc[(k - 1) & 1], a11);
            if (k > 0 && ((k - 1) & 3) == m) {
                const u32x4 ua = __builtin_bit_cast(u32x4, xa[(k - 1) & 1]), uc = __builtin_bit_cast(u32x4, xc[(k - 1) & 1]);
#pragma unroll
                for (int j = 0; j < 4; ++j) {
                    const unsigned a_ = ua[j], c_ = uc[j], g_ = gv[j];
                    pA = __builtin_amdgcn_fdot2_f32_bf16(__builtin_bit_cast(bf16x2, a_), __builtin_bit_cast(bf16x2, g_), pA, false);
                    pB = __builtin_amdgcn_fdot2_f32_bf16(__builtin_bit_cast(bf16x2, c_), __builtin_bit_cast(bf16x2, g_), pB, false);
                }
            }
            if (k < 32 && (k & 3) == 0) gv = *(LAS const u32x4*)(ring + 7168 + ((((k >> 2) + rot) & 7) * 64 + 16 * m + 8 * hi) * 2);
            __builtin_amdgcn_sched_barrier(0);
        }
        asm volatile("" : "+v"(a00), "+v"(a01), "+v"(a10), "+v"(a11));
        const int l3 = __builtin_amdgcn_mbcnt_hi(~0u, __builtin_amdgcn_mbcnt_lo(~0u, 0u));
        *(LAS float*)(ring + 6144 + 512 + l3 * 4) = pA; *(LAS float*)(ring + 6144 + 768 + l3 * 4) = pB;
    }
    int ln = __builtin_amdgcn_mbcnt_hi(~0u, __builtin_amdgcn_mbcnt_lo(~0u, 0u));
    asm volatile("" : "+v"(ln));
    const unsigned lo2 = ln * 16, stw2 = (ln >> 3) * 128 + (((ln & 7) ^ (((ln >> 4) & 1) << 2)) * 16);
    STAMP(1);
    lds_arrive(lds + L_MISC + 12, ln); lds_wait(lds + L_MISC + 12, 4);
    {
        zA = 0.f; zB = 0.f;
#pragma unroll
        for (int q = 0; q < 4; ++q) {
            lds_ptr pp = lds + L_B + q * 8192 + 6144 + 512 + (ln & 31) * 4;
            zA += *(LAS const float*)pp + *(LAS const float*)(pp + 128); zB += *(LAS const float*)(pp + 256) + *(LAS const float*)(pp + 384);
        }
        asm volatile("" : "+v"(zA), "+v"(zB));
    }
    lds_wait(lds + L_MISC + 24, 1);
    {
        const int head = xcd * 4 + (bi & 3);
        const char* kb = ws + WS_K + (size_t)head * 65536; const char* vb = ws + WS_V + (size_t)head * 65536;
#pragma unroll
        for (int j = 0; j < 8; ++j) { const int t = 32 + 8 * m + j; glds16_dev(kb + t * 1024, lo2, l0 + L_K + t * 1024); glds16_dev(vb + t * 1024, lo2, l0 + L_V + t * 1024); }
    }
    if (hact) {
#pragma unroll
        for (int i = 0; i < 16; ++i) { a00[i] = mish(a00[i]); a10[i] = mish(a10[i]); }
        const char* hd = ws + WS_H + (size_t)(2 * tg * 112 + 2 * cp) * 1024;
        gst_dev(hd, lo2, pack8(a00, 0)); gst_dev(hd + 1024, lo2, pack8(a00, 1)); gst_dev(hd + 2048, lo2, pack8(a10, 0)); gst_dev(hd + 3072, lo2, pack8(a10, 1));
#pragma unroll
        for (int i = 0; i < 16; ++i) { a01[i] = mish(a01[i]); a11[i] = mish(a11[i]); }
        hd += 112 * 1024;
        gst_dev(hd, lo2, pack8(a01, 0)); gst_dev(hd + 1024, lo2, pack8(a01, 1)); gst_dev(hd + 2048, lo2, pack8(a11, 0)); gst_dev(hd + 3072, lo2, pack8(a11, 1));
    }
    publish(ws, F_H + (tg * 32 + hg * 4 + m) * 8, tag, ln);
    lds_arrive(lds + L_MISC + 8, ln);
    STAMP(2);
    const unsigned w2vo = (ln >> 3) * 1024 + (ln & 7) * 16;
    const unsigned trp = ((ln & 15) >> 2) * 128 + (((ln >> 3) & 1) * 64) + ((ln >> 4) & 1) * 32 + (ln & 3) * 8 + 4 * (ln >> 5) * 128;
    {
        const char* w2s = w2 + (size_t)(28 * m) * 16384 + hg * 128;
        const char* hA = ws + WS_H + (size_t)(2 * tg * 112 + 28 * m) * 1024;
#pragma unroll
        for (int k = 0; k < 4; ++k) { GLD(st[2 * k], w2s + k * 16384, w2vo); GLD(st[2 * k + 1], w2s + k * 16384 + 8192, w2vo); }
        poll_tags(ws, F_H + tg * 256, 32, tag, ln);
        STAMP(3);
#pragma unroll
        for (int k = 0; k < 4; ++k) { GLD_DEV(st[8 + 2 * k], hA + k * 1024, lo2); GLD_DEV(st[9 + 2 * k], hA + (112 + k) * 1024, lo2); }
        WAITV(0);
        a00 = (f32x16){}; a01 = (f32x16){}; a10 = (f32x16){}; a11 = (f32x16){};
        auto bsteps = [&](int it, auto LAST) {
#pragma unroll
            for (int k = 0; k < 4; ++k) {
                asm volatile("s_waitcnt vmcnt(%4)" : "+v"(st[2 * k]), "+v"(st[2 * k + 1]), "+v"(st[8 + 2 * k]), "+v"(st[9 + 2 * k]) : "i"(LAST.value ? 12 - 4 * k : 12) : "memory");
                lds_ptr sl = ring + (k & 1) * 2048;
                *(LAS u32x4*)(sl + stw2) = st[2 * k]; *(LAS u32x4*)(sl + 1024 + stw2) = st[2 * k + 1];
                const bf16x8 f0 = trfrag(sl + trp, 1024), f1 = trfrag(sl + (trp ^ 64), 1024);
                const bf16x8 xa = asfrag(st[8 + 2 * k]), xc = asfrag(st[9 + 2 * k]);
                a00 = MFMA(f0, xa, a00); PACE(); a01 = MFMA(f0, xc, a01); PACE(); a10 = MFMA(f1, xa, a10); PACE(); a11 = MFMA(f1, xc, a11); PACE();
                if (!LAST.value) { const int kn = 4 * (it + 1) + k;
                    GLD(st[2 * k], w2s + kn * 16384, w2vo); GLD(st[2 * k + 1], w2s + kn * 16384 + 8192, w2vo);
                    GLD_DEV(st[8 + 2 * k], hA + kn * 1024, lo2); GLD_DEV(st[9 + 2 * k], hA + (112 + kn) * 1024, lo2); }
            } };
#pragma unroll 1
        for (int it = 0; it < 6; ++it) bsteps(it, std::false_type{});
        bsteps(6, std::true_type{});
        asm volatile("" : "+v"(a00), "+v"(a01), "+v"(a10), "+v"(a11));
    }
    STAMP(4);
    zA = __builtin_fmaxf(zA, -30.f); zB = __builtin_fmaxf(zB, -30.f);
    const float rhoA = __builtin_amdgcn_exp2f(-zA * LOG2E), rhoB = __builtin_amdgcn_exp2f(-zB * LOG2E);
    bf16x8 wf[10];
    {
        const char* wos = wo + (size_t)(8 * m) * 16384 + hg * 128;
#pragma unroll
        for (int k = 0; k < 8; ++k) { GLD(st[2 * k], wos + k * 16384, w2vo); GLD(st[2 * k + 1], wos + k * 16384 + 8192, w2vo); }
#pragma unroll
        for (int i = 0; i < 16; ++i) { a00[i] *= rhoA; a10[i] *= rhoA; a01[i] *= rhoB; a11[i] *= rhoB; }
        asm volatile("" : "+v"(a00), "+v"(a01), "+v"(a10), "+v"(a11));
#pragma unroll
        for (int k = 0; k < 8; ++k) {
            asm volatile("s_waitcnt vmcnt(%2)" : "+v"(st[2 * k]), "+v"(st[2 * k + 1]) : "i"(14 - 2 * k) : "memory");
            lds_ptr sl = ring + (k < 5 ? (k & 1) : (k - 5)) * 2048;
            *(LAS u32x4*)(sl + stw2) = st[2 * k]; *(LAS u32x4*)(sl + 1024 + stw2) = st[2 * k + 1];
            if (k < 5) { wf[2 * k] = trfrag(sl + trp, 1024); wf[2 * k + 1] = trfrag(sl + (trp ^ 64), 1024); }
        }
    }
#pragma unroll
    for (int tt = 0; tt < 2; ++tt) {
        poll_tags(ws, F_O + ((2 * tg + tt) * 32 + 8 * m) * 8, 8, tag, ln);
        const char* os = ws + WS_O + (size_t)((2 * tg + tt) * 32 + 8 * m) * 1024;
#pragma unroll
        for (int k = 0; k < 8; ++k) GLD_DEV(st[k], os + k * 1024, lo2);
#pragma unroll
        for (int k = 0; k < 8; ++k) {
            bf16x8 f0, f1;
            if (k < 5) { f0 = wf[2 * k]; f1 = wf[2 * k + 1]; } else { f0 = trfrag(ring + (k - 5) * 2048 + trp, 1024); f1 = trfrag(ring + (k - 5) * 2048 + (trp ^ 64), 1024); }
            asm volatile("s_waitcnt vmcnt(%1)" : "+v"(st[k]) : "i"(7 - k) : "memory");
            if (tt == 0) { a00 = MFMA(f0, asfrag(st[k]), a00); a10 = MFMA(f1, asfrag(st[k]), a10); }
            else         { a01 = MFMA(f0, asfrag(st[k]), a01); a11 = MFMA(f1, asfrag(st[k]), a11); }
        }
        STAMP(5 + tt);
    }
    BARRIER();
    if (b == 0 && m == 0 && ln == 0) __hip_atomic_store((GAS u64*)(ws + F_EPOCH), tag, __ATOMIC_RELAXED, __HIP_MEMORY_SCOPE_AGENT);
    auto put = [&](const f32x16& a, int ti) {
        if (ti != m) {
#pragma unroll
            for (int q = 0; q < 4; ++q) *(LAS f32x4*)(lds + ((ti * 4 + m) * 4 + q) * 1024 + lo2) = (f32x4){a[4 * q], a[4 * q + 1], a[4 * q + 2], a[4 * q + 3]};
        } };
    put(a00, 0); put(a01, 1); put(a10, 2); put(a11, 3);
    BARRIER();
    f32x16 acc = m == 0 ? a00 : m == 1 ? a01 : m == 2 ? a10 : a11;
#pragma unroll
    for (int o = 1; o < 4; ++o) {
        const int src = (m + o) & 3;
#pragma unroll
        for (int q = 0; q < 4; ++q) { const f32x4 v = *(LAS const f32x4*)(lds + ((m * 4 + src) * 4 + q) * 1024 + lo2); acc[4 * q] += v[0]; acc[4 * q + 1] += v[1]; acc[4 * q + 2] += v[2]; acc[4 * q + 3] += v[3]; }
    }
    const float g = __builtin_amdgcn_rcpf(1.f + ((m & 1) ? rhoB : rhoA));
    char* op = out + (size_t)(64 * tg + (m & 1) * 32 + (ln & 31)) * 1024 + hg * 128 + (m >> 1) * 64 + (ln >> 5) * 16;
#pragma unroll
    for (int p = 0; p < 2; ++p) {
        const unsigned a0 = cvtpk(acc[8 * p] * g, acc[8 * p + 1] * g), a1 = cvtpk(acc[8 * p + 2] * g, acc[8 * p + 3] * g);
        const unsigned b0 = cvtpk(acc[8 * p + 4] * g, acc[8 * p + 5] * g), b1v = cvtpk(acc[8 * p + 6] * g, acc[8 * p + 7] * g);
        auto r0 = __builtin_amdgcn_permlane32_swap(a0, b0, false, false);
        auto r1 = __builtin_amdgcn_permlane32_swap(a1, b1v, false, false);
        *(u32x4*)(op + p * 32) = (u32x4){r0[0], r1[0], r0[1], r1[1]};
    }
    STAMP(7);
    STAMP_FLUSH();
}

extern "C" void kernel_launch(void* const* d_in, const int* in_sizes, int n_in, void* d_out, int out_size, void* d_ws, size_t ws_size, hipStream_t stream) {
    (void)hipFuncSetAttribute((const void*)fused_kernel, hipFuncAttributeMaxDynamicSharedMemorySize, LDS_BYTES);
    fused_kernel<<<256, 768, LDS_BYTES, stream>>>((const char*)d_in[0], (const char*)d_in[1], (const char*)d_in[2], (const char*)d_in[4], (const unsigned*)d_in[5],
                                                  (const char*)d_in[6], (const char*)d_in[7], (char*)d_ws, (char*)d_out);
}
```

```cpp
#include <hip/hip_runtime.h>
#include <hip/hip_bf16.h>
#include <stdint.h>
#include <type_traits>

using bf16x8 = __attribute__((ext_vector_type(8))) short;
using s16x4  = __attribute__((ext_vector_type(4))) short;
using f32x16 = __attribute__((ext_vector_type(16))) float;
using f32x4  = __attribute__((ext_vector_type(4))) float;
using f32x2  = __attribute__((ext_vector_type(2))) float;
using u32x4  = __attribute__((ext_vector_type(4))) unsigned;
using u32x2  = __attribute__((ext_vector_type(2))) unsigned;
typedef __bf16 bf16x2 __attribute__((ext_vector_type(2)));
typedef short v4i16_t __attribute__((ext_vector_type(4)));
typedef unsigned long long u64;
#define DI __device__ __forceinline__
#define LAS __attribute__((address_space(3)))
#define GAS __attribute__((address_space(1)))
#define MFMA(a, b, c) __builtin_amdgcn_mfma_f32_32x32x16_bf16((a), (b), (c), 0, 0, 0)
typedef LAS char* lds_ptr;

constexpr unsigned WS_Q = 0, WS_K = 2u << 20, WS_V = 4u << 20, WS_O = 6u << 20, WS_H = 8u << 20, WS_F = 15u << 20;
constexpr unsigned WS_BYTES = 17u << 20;
#define FS 128
constexpr unsigned F_EPOCH = WS_F, F_QKV = WS_F + 4096, F_H = F_QKV + 256 * FS, F_O = F_H + 1024 * FS;
constexpr unsigned L_K = 0, L_V = 65536, L_B = 131072, L_MISC = L_B + 6144, LDS_BYTES = 163840;
#define FIRST_SLEEP 16
#define HGATE 0
#define PRIO_DROP 11
constexpr unsigned SPIN_CAP = 1u << 16;
constexpr float LOG2E = 1.4426950408889634f;
constexpr float QSCALE = 0.25f * LOG2E;

DI unsigned cvtpk(float lo, float hi) { return __builtin_bit_cast(unsigned, __builtin_convertvector((f32x2){lo, hi}, bf16x2)); }
DI u32x4 pack8(const f32x16& a, int s) { return (u32x4){cvtpk(a[8*s], a[8*s+1]), cvtpk(a[8*s+2], a[8*s+3]), cvtpk(a[8*s+4], a[8*s+5]), cvtpk(a[8*s+6], a[8*s+7])}; }
DI u32x4 srd(const void* p, unsigned bytes) { const u64 a = (u64)p; return (u32x4){(unsigned)a, (unsigned)(a >> 32) & 0xffffu, bytes, 0x00020000u}; }
DI void dma2(u32x4 rs, unsigned so, unsigned v0, unsigned v1, unsigned dst) {
    asm volatile("s_mov_b32 m0, %4\n\ts_nop 0\n\tbuffer_load_dwordx4 %0, %2, %3 offen lds\n\tbuffer_load_dwordx4 %1, %2, %3 offen offset:1024 lds"
                 :: "v"(v0), "v"(v1), "s"(rs), "s"(so), "s"(dst) : "memory"); }
DI void dma1(u32x4 rs, unsigned so, unsigned v0, unsigned dst) {
    asm volatile("s_mov_b32 m0, %3\n\ts_nop 0\n\tbuffer_load_dwordx4 %0, %1, %2 offen lds" :: "v"(v0), "s"(rs), "s"(so), "s"(dst) : "memory"); }
DI void dma4_dev(u32x4 rs, unsigned so, unsigned v0, unsigned dst) {
    asm volatile("s_mov_b32 m0, %3\n\ts_nop 0\n\tbuffer_load_dwordx4 %0, %1, %2 offen sc1 lds\n\tbuffer_load_dwordx4 %0, %1, %2 offen offset:1024 sc1 lds\n\t"
                 "buffer_load_dwordx4 %0, %1, %2 offen offset:2048 sc1 lds\n\tbuffer_load_dwordx4 %0, %1, %2 offen offset:3072 sc1 lds"
                 :: "v"(v0), "s"(rs), "s"(so), "s"(dst) : "memory"); }
DI void dma1_dev(u32x4 rs, unsigned so, unsigned v0, unsigned dst) {
    asm volatile("s_mov_b32 m0, %3\n\ts_nop 0\n\tbuffer_load_dwordx4 %0, %1, %2 offen sc1 lds" :: "v"(v0), "s"(rs), "s"(so), "s"(dst) : "memory"); }
DI void dma2_dev(u32x4 rs, unsigned so, unsigned v0, unsigned dst) {
    asm volatile("s_mov_b32 m0, %3\n\ts_nop 0\n\tbuffer_load_dwordx4 %0, %1, %2 offen sc1 lds\n\tbuffer_load_dwordx4 %0, %1, %2 offen offset:1024 sc1 lds"
                 :: "v"(v0), "s"(rs), "s"(so), "s"(dst) : "memory"); }
DI void BLD(u32x4& dst, u32x4 rs, unsigned so, unsigned vo) { asm volatile("buffer_load_dwordx4 %0, %1, %2, %3 offen" : "=v"(dst) : "v"(vo), "s"(rs), "s"(so) : "memory"); }
DI void BLD_DEV(u32x4& dst, u32x4 rs, unsigned so, unsigned vo) { asm volatile("buffer_load_dwordx4 %0, %1, %2, %3 offen sc1" : "=v"(dst) : "v"(vo), "s"(rs), "s"(so) : "memory"); }
DI void gst_tagged(u32x4 rs, unsigned so, unsigned vo, u32x4 v, unsigned tag) {
    const u32x4 a = {v[0], tag, v[1], tag}, b = {v[2], tag, v[3], tag};
    asm volatile("buffer_store_dwordx4 %0, %2, %3, %4 offen sc1\n\tbuffer_store_dwordx4 %1, %2, %3, %4 offen offset:1024 sc1\n\ts_nop 1" :: "v"(a), "v"(b), "v"(vo), "s"(rs), "s"(so) : "memory"); }
DI void gst_plain_dev(u32x4 rs, unsigned so, unsigned vo, u32x4 v) {
    asm volatile("buffer_store_dwordx4 %0, %1, %2, %3 offen sc1\n\ts_nop 1" :: "v"(v), "v"(vo), "s"(rs), "s"(so) : "memory"); }
DI void ld_tagged(u32x4& a, u32x4& b, u32x4 rs, unsigned so, unsigned vo) {
    asm volatile("buffer_load_dwordx4 %0, %2, %3, %4 offen sc1\n\tbuffer_load_dwordx4 %1, %2, %3, %4 offen offset:1024 sc1" : "=&v"(a), "=&v"(b) : "v"(vo), "s"(rs), "s"(so) : "memory"); }
DI bool tags_ok(const u32x4& a, const u32x4& b, unsigned tag) { return __all(((a[1] ^ tag) | (a[3] ^ tag) | (b[1] ^ tag) | (b[3] ^ tag)) == 0u); }
DI void GLD(u32x4& dst, const void* sbase, unsigned voff) { asm volatile("global_load_dwordx4 %0, %1, %2" : "=v"(dst) : "v"(voff), "s"(sbase) : "memory"); }
DI void GLD_DEV(u32x4& dst, const void* sbase, unsigned voff) { asm volatile("global_load_dwordx4 %0, %1, %2 sc1" : "=v"(dst) : "v"(voff), "s"(sbase) : "memory"); }
DI void gst_dev(const void* sbase, unsigned voff, u32x4 v) { asm volatile("global_store_dwordx4 %0, %1, %2 sc1\n\ts_nop 1" :: "v"(voff), "v"(v), "s"(sbase) : "memory"); }
DI s16x4 trd(lds_ptr p) { return __builtin_bit_cast(s16x4, __builtin_amdgcn_ds_read_tr16_b64_v4i16((LAS v4i16_t*)p)); }
DI bf16x8 trfrag(lds_ptr p, int second) { s16x4 a = trd(p), b = trd(p + second); return __builtin_shufflevector(a, b, 0, 1, 2, 3, 4, 5, 6, 7); }
DI bf16x8 ldfrag(lds_ptr p) { return *(LAS const bf16x8*)p; }
DI bf16x8 asfrag(u32x4 v) { return __builtin_bit_cast(bf16x8, v); }
#define PACE_N 0
#define PACE() do { __builtin_amdgcn_sched_barrier(0); if (PACE_N) __builtin_amdgcn_s_sleep(PACE_N); __builtin_amdgcn_sched_barrier(0); } while (0)
#define WAITV(n) asm volatile("s_waitcnt vmcnt(%0)" :: "i"(n) : "memory")
#define BARRIER() do { asm volatile("" ::: "memory"); __builtin_amdgcn_s_waitcnt(0xC07F); __builtin_amdgcn_s_barrier(); asm volatile("" ::: "memory"); } while (0)
DI unsigned mish2(float h0, float h1) {
    const f32x2 h = {h0, h1}, e = h * LOG2E;
    const f32x2 u = {__builtin_amdgcn_exp2f(e[0]), __builtin_amdgcn_exp2f(e[1])};
    const f32x2 two = {2.f, 2.f}, d = __builtin_elementwise_fma(u, u + two, two);
    const f32x2 r = {__builtin_amdgcn_rcpf(d[0]), __builtin_amdgcn_rcpf(d[1])};
    const f32x2 y = h * __builtin_elementwise_fma(r, (f32x2){-2.f, -2.f}, (f32x2){1.f, 1.f});
    return cvtpk(y[0], y[1]);
}
DI u32x4 mish8(const f32x16& a, int s) { return (u32x4){mish2(a[8*s], a[8*s+1]), mish2(a[8*s+2], a[8*s+3]), mish2(a[8*s+4], a[8*s+5]), mish2(a[8*s+6], a[8*s+7])}; }
DI float halfmax(float m) { auto rr = __builtin_amdgcn_permlane32_swap(__float_as_uint(m), __float_as_uint(m), false, false); return __builtin_fmaxf(__uint_as_float(rr[0]), __uint_as_float(rr[1])); }
DI float max16(const f32x16& p) {
    float a = __builtin_fmaxf(__builtin_fmaxf(p[0], p[1]), p[2]), b = __builtin_fmaxf(__builtin_fmaxf(p[3], p[4]), p[5]);
    a = __builtin_fmaxf(__builtin_fmaxf(a, p[6]), p[7]); b = __builtin_fmaxf(__builtin_fmaxf(b, p[8]), p[9]);
    a = __builtin_fmaxf(__builtin_fmaxf(a, p[10]), p[11]); b = __builtin_fmaxf(__builtin_fmaxf(b, p[12]), p[13]);
    a = __builtin_fmaxf(__builtin_fmaxf(a, p[14]), p[15]); return __builtin_fmaxf(a, b); }
DI void publish(char* ws, unsigned off, u64 tag, int lane) {
    asm volatile("s_waitcnt vmcnt(0)" ::: "memory");
    if (lane == 0) __hip_atomic_store((GAS u64*)(ws + off), tag, __ATOMIC_RELAXED, __HIP_MEMORY_SCOPE_AGENT);
}
DI void poll_tags(char* ws, unsigned off, int n, u64 tag, int lane) {
    GAS u64* p = (GAS u64*)(ws + off + (lane < n ? lane : 0) * FS);
#pragma unroll 1
    for (unsigned s = 0; s < SPIN_CAP; ++s) {
        const u64 v = __hip_atomic_load(p, __ATOMIC_RELAXED, __HIP_MEMORY_SCOPE_AGENT);
        if (__all(v == tag)) break;
        __builtin_amdgcn_s_sleep(2);
    }
    asm volatile("" ::: "memory");
}
DI void lds_arrive(lds_ptr c, int lane) {
    asm volatile("s_waitcnt lgkmcnt(0)" ::: "memory");
    if (lane == 0) __hip_atomic_fetch_add((LAS unsigned*)c, 1u, __ATOMIC_RELAXED, __HIP_MEMORY_SCOPE_WORKGROUP);
}
DI void lds_wait(lds_ptr c, unsigned n) {
#pragma unroll 1
    for (unsigned s = 0; s < SPIN_CAP * 8; ++s) {
        const unsigned v = __builtin_amdgcn_readfirstlane(__hip_atomic_load((LAS unsigned*)c, __ATOMIC_RELAXED, __HIP_MEMORY_SCOPE_WORKGROUP));
        if (v >= n) break;
        __builtin_amdgcn_s_sleep(1);
    }
    asm volatile("" ::: "memory");
}
#define STAMP(i)
#define CSTAMP(i)
#define STAMP_DECL()
#define STAMP_FLUSH()
DI unsigned ximg(int kc) { return 32768u + (kc & 3) * 8192u + (kc >> 2) * 65536u; }
constexpr unsigned wimg(int n) { return n < 4 ? L_B + n * 8192u : n < 8 ? L_K + (n - 4) * 8192u : L_V + (n - 8) * 8192u; }

__global__ __launch_bounds__(768, 3) void fused_kernel(const char* __restrict__ x, const char* __restrict__ wqkv, const char* __restrict__ wo,
                                                       const char* __restrict__ w1, const unsigned* __restrict__ b1, const char* __restrict__ w2,
                                                       const char* __restrict__ gw, char* __restrict__ ws, char* __restrict__ out) {
    extern __shared__ __attribute__((aligned(16))) char lds_raw[];
    lds_ptr lds = (lds_ptr)lds_raw;
    const unsigned l0 = (unsigned)(uintptr_t)lds_raw;
    const int tid = threadIdx.x, lane = tid & 63, w = __builtin_amdgcn_readfirstlane(tid >> 6), hi = lane >> 5;
    const int b = blockIdx.x, xcd = b & 7, bi = b >> 3;
    const int tg = (xcd >> 1) * 8 + (bi & 7), hg = (xcd & 1) * 4 + (bi >> 3);
    const int rot = ((bi & 7) + 2 * (bi >> 3)) & 7;
    const int head = xcd * 4 + (bi & 3);
    const unsigned lo16 = lane * 16;
    const unsigned xfo = (lane & 31) * 128 + ((hi ^ ((lane >> 1) & 7)) * 16);
    STAMP_DECL();
    STAMP(0);
    if (w < 8) {
        const u32x4 rws = srd(ws, WS_BYTES);
        const int ct = (w < 4) ? (w & 1) : ((w >> 1) & 1);
        const unsigned wfo = (8 * hi + ((lane & 15) >> 2)) * 128 + ((ct ^ ((lane >> 3) & 1)) * 64) + ((lane >> 4) & 1) * 32 + (lane & 3) * 8;
        f32x16 acc0 = {}, acc1 = {};
        u64 tag;
        if (w < 4) {
            const unsigned sgo = (w >> 1) * 8192u;
#pragma unroll
            for (int i = 0; i < 8; ++i) {
                BARRIER();
                if (i == 0 || i == 7) STAMP(8 + i);
                const unsigned xb = ximg((i + rot) & 7) + xfo;
                const unsigned wb = ((i & 3) == 1 ? (w < 2 ? wimg(3) : wimg(4)) : wimg((i & 3) * 3) + sgo) + wfo;
                bf16x8 wf[4], xa[4], xc[4];
#pragma unroll
                for (int ks = 0; ks < 4; ++ks) { wf[ks] = trfrag(lds + wb + ks * 2048, 512); xa[ks] = ldfrag(lds + (xb ^ (ks * 32))); xc[ks] = ldfrag(lds + (xb ^ (ks * 32)) + 4096); }
                if (i == 7) { tag = *(LAS const u64*)(lds + L_MISC + 16); BARRIER(); }
                __builtin_amdgcn_sched_barrier(0);
#pragma unroll
                for (int ks = 0; ks < 4; ++ks) { acc0 = MFMA(wf[ks], xa[ks], acc0); acc1 = MFMA(wf[ks], xc[ks], acc1); }
            }
            const unsigned fr = (4 * hg + 2 * ct) * 64 + 2 * tg;
            if (w < 2) {
#pragma unroll
                for (int i = 0; i < 16; ++i) { acc0[i] *= QSCALE; acc1[i] *= QSCALE; }
            }
            const unsigned so = (w < 2 ? WS_Q : WS_K) + fr * 1024;
            gst_plain_dev(rws, so, lo16, pack8(acc0, 0)); gst_plain_dev(rws, so + 1024, lo16, pack8(acc1, 0));
            gst_plain_dev(rws, so + 65536, lo16, pack8(acc0, 1)); gst_plain_dev(rws, so + 65536 + 1024, lo16, pack8(acc1, 1));
        } else {
            const int tt = w & 1;
#pragma unroll
            for (int i = 0; i < 8; ++i) {
                BARRIER();
                if (i == 0 || i == 7) STAMP(8 + i);
                const unsigned xb = ximg((i + rot) & 7) + xfo + tt * 4096;
                bf16x8 wf[4], xa[4];
#pragma unroll
                for (int ks = 0; ks < 4; ++ks) { wf[ks] = trfrag(lds + wimg((i & 3) * 3 + 2) + wfo + ks * 2048, 512); xa[ks] = ldfrag(lds + (xb ^ (ks * 32))); }
                if (i == 7) { tag = *(LAS const u64*)(lds + L_MISC + 16); BARRIER(); }
                __builtin_amdgcn_sched_barrier(0);
#pragma unroll
                for (int ks = 0; ks < 4; ++ks) acc0 = MFMA(xa[ks], wf[ks], acc0);
            }
            const unsigned vo = hi * 256 + (lane & 15) * 16, h2 = (lane >> 4) & 1;
            const unsigned so = WS_V + ((4 * hg + 2 * ct) * 64 + 2 * tg + tt) * 1024;
            gst_plain_dev(rws, so, vo + h2 * 65536, pack8(acc0, 0)); gst_plain_dev(rws, so, vo + h2 * 65536 + 512, pack8(acc0, 1));
        }
        STAMP(1);
        WAITV(0);
        STAMP(6);
        if (__builtin_amdgcn_readfirstlane(lane == 0 ? __hip_atomic_fetch_add((LAS unsigned*)(lds + L_MISC + 24), 1u, __ATOMIC_RELAXED, __HIP_MEMORY_SCOPE_WORKGROUP) : 0u) == 7u) {
            if (lane == 0) __hip_atomic_store((GAS u64*)(ws + F_QKV + (hg * 32 + tg) * FS), tag, __ATOMIC_RELAXED, __HIP_MEMORY_SCOPE_AGENT);
            poll_tags(ws, F_QKV + (head >> 2) * 32 * FS, 32, tag, lane);
            if (lane == 0) __hip_atomic_store((LAS unsigned*)(lds + L_MISC + 28), 1u, __ATOMIC_RELAXED, __HIP_MEMORY_SCOPE_WORKGROUP);
        } else lds_wait(lds + L_MISC + 28, 1);
        STAMP(2);
        const int tile = 8 * (bi >> 2) + (w < 4 ? 2 * w : 2 * (w - 4) + 1);
        u32x4 qr;
        BLD_DEV(qr, rws, WS_Q + (head * 64 + tile) * 1024, lo16);
#pragma unroll
        for (int j = 0; j < 3; ++j) { dma1_dev(rws, WS_K + (head * 64 + j * 8 + w) * 1024, lo16, l0 + L_K + (j * 8 + w) * 1024); dma1_dev(rws, WS_V + (head * 64 + j * 8 + w) * 1024, lo16, l0 + L_V + (j * 8 + w) * 1024); }
        asm volatile("s_waitcnt vmcnt(4)" : "+v"(qr) :: "memory");
        const bf16x8 qf = asfrag(qr);
        lds_arrive(lds + L_MISC, lane); lds_wait(lds + L_MISC, 8);
        STAMP(3); CSTAMP(10);
        if (w >= 4) __builtin_amdgcn_s_setprio(1);
        const f32x16 zero = {};
        const float nm = -halfmax(max16(MFMA(ldfrag(lds + L_K + lo16), qf, zero)));
        float o[9];
        {
            unsigned ka = l0 + L_K + lo16, va = l0 + L_V + hi * 256 + (lane & 15) * 16; const unsigned cnt_addr = l0 + L_MISC;
            unsigned s_it, s_sp, s_st;
            const unsigned onev = ((lane & 27) == 16) ? 0x3f803f80u : 0u;
    asm volatile(
        "ds_read_b128 v[136:139], %[ka]\n\t"
        "ds_read_b128 v[140:143], %[ka] offset:1024\n\t"
        "v_mov_b32 v144, %[one]\n\t"
        "v_mov_b32 v148, %[one]\n\t"
        "v_mov_b32 v145, %[one]\n\t"
        "v_mov_b32 v149, %[one]\n\t"
        "v_mov_b32 v146, %[one]\n\t"
        "v_mov_b32 v150, %[one]\n\t"
        "v_mov_b32 v147, %[one]\n\t"
        "v_mov_b32 v151, %[one]\n\t"
        "v_mov_b32 v152, %[one]\n\t"
        "v_mov_b32 v156, %[one]\n\t"
        "v_mov_b32 v153, %[one]\n\t"
        "v_mov_b32 v157, %[one]\n\t"
        "v_mov_b32 v154, %[one]\n\t"
        "v_mov_b32 v158, %[one]\n\t"
        "v_mov_b32 v155, %[one]\n\t"
        "v_mov_b32 v159, %[one]\n\t"
        "s_mov_b64 exec, %[vm]\n\t"
        "ds_read_b128 v[144:147], %[va] offset:0\n\t"
        "ds_read_b128 v[148:151], %[va] offset:512\n\t"
        "s_mov_b64 exec, -1\n\t"
        "v_mov_b32 v112, %[nm]\n\t"
        "v_mov_b32 v113, %[nm]\n\t"
        "v_mov_b32 v114, %[nm]\n\t"
        "v_mov_b32 v115, %[nm]\n\t"
        "v_mov_b32 v116, %[nm]\n\t"
        "v_mov_b32 v117, %[nm]\n\t"
        "v_mov_b32 v118, %[nm]\n\t"
        "v_mov_b32 v119, %[nm]\n\t"
        "v_mov_b32 v120, %[nm]\n\t"
        "v_mov_b32 v121, %[nm]\n\t"
        "v_mov_b32 v122, %[nm]\n\t"
        "v_mov_b32 v123, %[nm]\n\t"
        "v_mov_b32 v124, %[nm]\n\t"
        "v_mov_b32 v125, %[nm]\n\t"
        "v_mov_b32 v126, %[nm]\n\t"
        "v_mov_b32 v127, %[nm]\n\t"
        "v_mov_b32 v96, 0\n\t"
        "v_mov_b32 v97, 0\n\t"
        "v_mov_b32 v98, 0\n\t"
        "v_mov_b32 v99, 0\n\t"
        "v_mov_b32 v100, 0\n\t"
        "v_mov_b32 v101, 0\n\t"
        "v_mov_b32 v102, 0\n\t"
        "v_mov_b32 v103, 0\n\t"
        "v_mov_b32 v104, 0\n\t"
        "v_mov_b32 v105, 0\n\t"
        "v_mov_b32 v106, 0\n\t"
        "v_mov_b32 v107, 0\n\t"
        "v_mov_b32 v108, 0\n\t"
        "v_mov_b32 v109, 0\n\t"
        "v_mov_b32 v110, 0\n\t"
        "v_mov_b32 v111, 0\n\t"
        "v_mov_b32 v132, 0\n\t"
        "v_mov_b32 v133, 0\n\t"
        "v_mov_b32 v134, 0\n\t"
        "v_mov_b32 v135, 0\n\t"
        "s_mov_b32 %[it], 0\n\t"
        "s_waitcnt lgkmcnt(3)\n\t"
        "v_mfma_f32_32x32x16_bf16 v[64:79], v[136:139], %[q], v[112:127]\n\t"
        "1:\n\t"
        "s_cmp_eq_u32 %[it], 1\n\t"
        "s_cbranch_scc1 3f\n\t"
        "s_cmp_eq_u32 %[it], 5\n\t"
        "s_cbranch_scc1 4f\n\t"
        "s_cmp_lg_u32 %[it], %[pd]\n\t"
        "s_cbranch_scc1 2f\n\t"
        "s_setprio 0\n\t"
        "2:\n\t"
        "s_waitcnt lgkmcnt(2)\n\t"
        "v_mfma_f32_32x32x16_bf16 v[80:95], v[140:143], %[q], v[112:127]\n\t"
        "ds_read_b128 v[136:139], %[ka] offset:2048\n\t"
        "v_exp_f32 v64, v64\n\t"
        "v_exp_f32 v65, v65\n\t"
        "v_exp_f32 v66, v66\n\t"
        "v_exp_f32 v67, v67\n\t"
        "v_exp_f32 v68, v68\n\t"
        "v_exp_f32 v69, v69\n\t"
        "v_mfma_f32_32x32x16_bf16 v[96:111], v[156:159], v[132:135], v[96:111]\n\t"
        "s_mov_b64 exec, %[vm]\n\t"
        "ds_read_b128 v[152:155], %[va] offset:1024\n\t"
        "ds_read_b128 v[156:159], %[va] offset:1536\n\t"
        "s_mov_b64 exec, -1\n\t"
        "v_exp_f32 v70, v70\n\t"
        "v_exp_f32 v71, v71\n\t"
        "v_cvt_pk_bf16_f32 v128, v64, v65\n\t"
        "v_cvt_pk_bf16_f32 v129, v66, v67\n\t"
        "v_cvt_pk_bf16_f32 v130, v68, v69\n\t"
        "v_cvt_pk_bf16_f32 v131, v70, v71\n\t"
        "v_exp_f32 v72, v72\n\t"
        "v_exp_f32 v73, v73\n\t"
        "v_exp_f32 v74, v74\n\t"
        "s_waitcnt lgkmcnt(3)\n\t"
        "v_mfma_f32_32x32x16_bf16 v[96:111], v[144:147], v[128:131], v[96:111]\n\t"
        "v_exp_f32 v75, v75\n\t"
        "v_exp_f32 v76, v76\n\t"
        "v_exp_f32 v77, v77\n\t"
        "v_exp_f32 v78, v78\n\t"
        "v_exp_f32 v79, v79\n\t"
        "v_cvt_pk_bf16_f32 v132, v72, v73\n\t"
        "v_cvt_pk_bf16_f32 v133, v74, v75\n\t"
        "v_cvt_pk_bf16_f32 v134, v76, v77\n\t"
        "v_cvt_pk_bf16_f32 v135, v78, v79\n\t"
        "s_waitcnt lgkmcnt(2)\n\t"
        "v_mfma_f32_32x32x16_bf16 v[64:79], v[136:139], %[q], v[112:127]\n\t"
        "ds_read_b128 v[140:143], %[ka] offset:3072\n\t"
        "v_exp_f32 v80, v80\n\t"
        "v_exp_f32 v81, v81\n\t"
        "v_exp_f32 v82, v82\n\t"
        "v_exp_f32 v83, v83\n\t"
        "v_exp_f32 v84, v84\n\t"
        "v_exp_f32 v85, v85\n\t"
        "v_mfma_f32_32x32x16_bf16 v[96:111], v[148:151], v[132:135], v[96:111]\n\t"
        "s_mov_b64 exec, %[vm]\n\t"
        "ds_read_b128 v[144:147], %[va] offset:2048\n\t"
        "ds_read_b128 v[148:151], %[va] offset:2560\n\t"
        "s_mov_b64 exec, -1\n\t"
        "v_exp_f32 v86, v86\n\t"
        "v_exp_f32 v87, v87\n\t"
        "v_cvt_pk_bf16_f32 v128, v80, v81\n\t"
        "v_cvt_pk_bf16_f32 v129, v82, v83\n\t"
        "v_cvt_pk_bf16_f32 v130, v84, v85\n\t"
        "v_cvt_pk_bf16_f32 v131, v86, v87\n\t"
        "v_exp_f32 v88, v88\n\t"
        "v_exp_f32 v89, v89\n\t"
        "v_exp_f32 v90, v90\n\t"
        "s_waitcnt lgkmcnt(3)\n\t"
        "v_mfma_f32_32x32x16_bf16 v[96:111], v[152:155], v[128:131], v[96:111]\n\t"
        "v_exp_f32 v91, v91\n\t"
        "v_exp_f32 v92, v92\n\t"
        "v_exp_f32 v93, v93\n\t"
        "v_exp_f32 v94, v94\n\t"
        "v_exp_f32 v95, v95\n\t"
        "v_cvt_pk_bf16_f32 v132, v88, v89\n\t"
        "v_cvt_pk_bf16_f32 v133, v90, v91\n\t"
        "v_cvt_pk_bf16_f32 v134, v92, v93\n\t"
        "v_cvt_pk_bf16_f32 v135, v94, v95\n\t"
        "s_waitcnt lgkmcnt(2)\n\t"
        "v_mfma_f32_32x32x16_bf16 v[80:95], v[140:143], %[q], v[112:127]\n\t"
        "ds_read_b128 v[136:139], %[ka] offset:4096\n\t"
        "v_exp_f32 v64, v64\n\t"
        "v_exp_f32 v65, v65\n\t"
        "v_exp_f32 v66, v66\n\t"
        "v_exp_f32 v67, v67\n\t"
        "v_exp_f32 v68, v68\n\t"
        "v_exp_f32 v69, v69\n\t"
        "v_mfma_f32_32x32x16_bf16 v[96:111], v[156:159], v[132:135], v[96:111]\n\t"
        "s_mov_b64 exec, %[vm]\n\t"
        "ds_read_b128 v[152:155], %[va] offset:3072\n\t"
        "ds_read_b128 v[156:159], %[va] offset:3584\n\t"
        "s_mov_b64 exec, -1\n\t"
        "v_exp_f32 v70, v70\n\t"
        "v_exp_f32 v71, v71\n\t"
        "v_cvt_pk_bf16_f32 v128, v64, v65\n\t"
        "v_cvt_pk_bf16_f32 v129, v66, v67\n\t"
        "v_cvt_pk_bf16_f32 v130, v68, v69\n\t"
        "v_cvt_pk_bf16_f32 v131, v70, v71\n\t"
        "v_exp_f32 v72, v72\n\t"
        "v_exp_f32 v73, v73\n\t"
        "v_exp_f32 v74, v74\n\t"
        "s_waitcnt lgkmcnt(3)\n\t"
        "v_mfma_f32_32x32x16_bf16 v[96:111], v[144:147], v[128:131], v[96:111]\n\t"
        "v_exp_f32 v75, v75\n\t"
        "v_exp_f32 v76, v76\n\t"
        "v_exp_f32 v77, v77\n\t"
        "v_exp_f32 v78, v78\n\t"
        "v_exp_f32 v79, v79\n\t"
        "v_cvt_pk_bf16_f32 v132, v72, v73\n\t"
        "v_cvt_pk_bf16_f32 v133, v74, v75\n\t"
        "v_cvt_pk_bf16_f32 v134, v76, v77\n\t"
        "v_cvt_pk_bf16_f32 v135, v78, v79\n\t"
        "s_waitcnt lgkmcnt(2)\n\t"
        "v_mfma_f32_32x32x16_bf16 v[64:79], v[136:139], %[q], v[112:127]\n\t"
        "ds_read_b128 v[140:143], %[ka] offset:5120\n\t"
        "v_exp_f32 v80, v80\n\t"
        "v_exp_f32 v81, v81\n\t"
        "v_exp_f32 v82, v82\n\t"
        "v_exp_f32 v83, v83\n\t"
        "v_exp_f32 v84, v84\n\t"
        "v_exp_f32 v85, v85\n\t"
        "v_mfma_f32_32x32x16_bf16 v[96:111], v[148:151], v[132:135], v[96:111]\n\t"
        "s_mov_b64 exec, %[vm]\n\t"
        "ds_read_b128 v[144:147], %[va] offset:4096\n\t"
        "ds_read_b128 v[148:151], %[va] offset:4608\n\t"
        "s_mov_b64 exec, -1\n\t"
        "v_exp_f32 v86, v86\n\t"
        "v_exp_f32 v87, v87\n\t"
        "v_cvt_pk_bf16_f32 v128, v80, v81\n\t"
        "v_cvt_pk_bf16_f32 v129, v82, v83\n\t"
        "v_cvt_pk_bf16_f32 v130, v84, v85\n\t"
        "v_cvt_pk_bf16_f32 v131, v86, v87\n\t"
        "v_exp_f32 v88, v88\n\t"
        "v_exp_f32 v89, v89\n\t"
        "v_exp_f32 v90, v90\n\t"
        "s_waitcnt lgkmcnt(3)\n\t"
        "v_mfma_f32_32x32x16_bf16 v[96:111], v[152:155], v[128:131], v[96:111]\n\t"
        "v_exp_f32 v91, v91\n\t"
        "v_exp_f32 v92, v92\n\t"
        "v_exp_f32 v93, v93\n\t"
        "v_exp_f32 v94, v94\n\t"
        "v_exp_f32 v95, v95\n\t"
        "v_add_u32 %[ka], 0x1000, %[ka]\n\t"
        "v_add_u32 %[va], 0x1000, %[va]\n\t"
        "v_cvt_pk_bf16_f32 v132, v88, v89\n\t"
        "v_cvt_pk_bf16_f32 v133, v90, v91\n\t"
        "v_cvt_pk_bf16_f32 v134, v92, v93\n\t"
        "v_cvt_pk_bf16_f32 v135, v94, v95\n\t"
        "s_add_u32 %[it], %[it], 1\n\t"
        "s_cmp_lt_u32 %[it], 16\n\t"
        "s_cbranch_scc1 1b\n\t"
        "s_branch 9f\n\t"
        "3:\n\t"
        "s_waitcnt vmcnt(0)\n\t"
        "v_mov_b32 v160, 1\n\t"
        "s_mov_b64 exec, 1\n\t"
        "ds_add_u32 %[cn], v160 offset:4\n\t"
        "s_mov_b64 exec, -1\n\t"
        "s_mov_b32 %[sp], 0\n\t"
        "5:\n\t"
        "ds_read_b32 v160, %[cn] offset:4\n\t"
        "s_waitcnt lgkmcnt(0)\n\t"
        "v_readfirstlane_b32 %[st], v160\n\t"
        "s_add_u32 %[sp], %[sp], 1\n\t"
        "s_cmp_ge_u32 %[st], 8\n\t"
        "s_cbranch_scc1 2b\n\t"
        "s_sleep 1\n\t"
        "s_cmp_lt_u32 %[sp], %[cap]\n\t"
        "s_cbranch_scc1 5b\n\t"
        "s_branch 2b\n\t"
        "4:\n\t"
        "s_mov_b32 %[sp], 0\n\t"
        "6:\n\t"
        "ds_read_b32 v160, %[cn] offset:8\n\t"
        "s_waitcnt lgkmcnt(0)\n\t"
        "v_readfirstlane_b32 %[st], v160\n\t"
        "s_add_u32 %[sp], %[sp], 1\n\t"
        "s_cmp_ge_u32 %[st], 4\n\t"
        "s_cbranch_scc1 2b\n\t"
        "s_sleep 2\n\t"
        "s_cmp_lt_u32 %[sp], %[cap]\n\t"
        "s_cbranch_scc1 6b\n\t"
        "s_branch 2b\n\t"
        "9:\n\t"
        "s_nop 1\n\t"
        "v_mfma_f32_32x32x16_bf16 v[96:111], v[156:159], v[132:135], v[96:111]\n\t"
        "s_waitcnt lgkmcnt(0)\n\t"
        "s_nop 15\n\t"
        "v_mov_b32 %[o0], v96\n\t"
        "v_mov_b32 %[o1], v97\n\t"
        "v_mov_b32 %[o2], v98\n\t"
        "v_mov_b32 %[o3], v99\n\t"
        "v_mov_b32 %[o4], v100\n\t"
        "v_mov_b32 %[o5], v101\n\t"
        "v_mov_b32 %[o6], v102\n\t"
        "v_mov_b32 %[o7], v103\n\t"
        "v_mov_b32 %[o8], v104\n\t"
        : [o0] "=&v"(o[0]), [o1] "=&v"(o[1]), [o2] "=&v"(o[2]), [o3] "=&v"(o[3]), [o4] "=&v"(o[4]), [o5] "=&v"(o[5]), [o6] "=&v"(o[6]), [o7] "=&v"(o[7]), [o8] "=&v"(o[8]), [ka] "+v"(ka), [va] "+v"(va), [it] "=&s"(s_it), [sp] "=&s"(s_sp), [st] "=&s"(s_st)
        : [q] "v"(qf), [nm] "v"(nm), [one] "v"(onev), [vm] "s"(0x0000ffff0000ffffull), [cn] "v"(cnt_addr), [cap] "s"(SPIN_CAP), [pd] "i"(PRIO_DROP)
        : "memory", "scc", "v64", "v65", "v66", "v67", "v68", "v69", "v70", "v71", "v72", "v73", "v74", "v75", "v76", "v77", "v78", "v79", "v80", "v81", "v82", "v83", "v84", "v85", "v86", "v87", "v88", "v89", "v90", "v91", "v92", "v93", "v94", "v95", "v96", "v97", "v98", "v99", "v100", "v101", "v102", "v103", "v104", "v105", "v106", "v107", "v108", "v109", "v110", "v111", "v112", "v113", "v114", "v115", "v116", "v117", "v118", "v119", "v120", "v121", "v122", "v123", "v124", "v125", "v126", "v127", "v128", "v129", "v130", "v131", "v132", "v133", "v134", "v135", "v136", "v137", "v138", "v139", "v140", "v141", "v142", "v143", "v144", "v145", "v146", "v147", "v148", "v149", "v150", "v151", "v152", "v153", "v154", "v155", "v156", "v157", "v158", "v159", "v160");
        }
        STAMP(4); CSTAMP(11);
        float big = __builtin_fabsf(o[8]);
#pragma unroll
        for (int i = 0; i < 8; ++i) big = __builtin_fmaxf(big, __builtin_fabsf(o[i]) * 0x1p-60f);
        if (__builtin_expect(__any(!(big < 0x1p60f) || !(o[8] > 0x1p-20f)), 0)) {
            float mr = -INFINITY; f32x16 oo = zero;
            const bf16x8 ones = asfrag((u32x4){0x3f803f80u, 0x3f803f80u, 0x3f803f80u, 0x3f803f80u});
#pragma unroll 1
            for (int t = 0; t < 64; ++t) {
                f32x16 s = MFMA(ldfrag(lds + L_K + t * 1024 + lo16), qf, zero);
                const float mn = __builtin_fmaxf(mr, halfmax(max16(s)));
                const float al = __builtin_amdgcn_exp2f(mr - mn); mr = mn;
#pragma unroll
                for (int i = 0; i < 16; ++i) { oo[i] *= al; s[i] = __builtin_amdgcn_exp2f(s[i] - mn); }
                lds_ptr vp = lds + L_V + t * 1024 + hi * 256 + (lane & 15) * 16;
                const bf16x8 v0 = ldfrag(vp), v1 = ldfrag(vp + 512);
                oo = MFMA((lane & 16) ? ones : v0, asfrag(pack8(s, 0)), oo);
                oo = MFMA((lane & 16) ? ones : v1, asfrag(pack8(s, 1)), oo);
            }
#pragma unroll
            for (int i = 0; i < 9; ++i) o[i] = oo[i];
        }
        const float rl = __builtin_amdgcn_rcpf(o[8]);
        gst_plain_dev(rws, WS_O + (tile * 32 + head) * 1024, lo16,
                      (u32x4){cvtpk(o[0] * rl, o[1] * rl), cvtpk(o[2] * rl, o[3] * rl), cvtpk(o[4] * rl, o[5] * rl), cvtpk(o[6] * rl, o[7] * rl)});
        publish(ws, F_O + (tile * 32 + head) * FS, tag, lane);
        STAMP(5);
        STAMP_FLUSH();
        __builtin_amdgcn_endpgm();
    }
    const int m = w - 8;
    lds_ptr ring = lds + L_B + m * 8192;
    const int cp = (hg >> 1) * 14 + (hg & 1) * 8 + 2 * m;
    const bool hact = m < ((hg & 1) ? 3 : 4);
    const unsigned swz8 = (lane >> 3) * 16, swzc = ((lane & 7) ^ (((lane >> 4) & 1) << 2)) * 16;
    u64 tag;
    f32x16 a00, a01, a10, a11;
    const u32x4 rw1 = srd(w1 + (hact ? cp : 0) * 64, 512 * 3584);
    const unsigned w1v0 = (lane >> 3) * 3584 + swzc, w1v1 = w1v0 + 8 * 3584 - 1024;
    auto hslot = [&](int k) -> unsigned { const int s = k % 5; return s < 3 ? L_B + m * 8192 + s * 2048 : (s == 3 ? L_K : L_V) + 24576 + m * 2048; };
    auto w1dma = [&](int k) { dma2(rw1, (((((k >> 2) + rot) & 7) << 2) + (((k & 3) + m) & 3)) * 57344, w1v0, w1v1, l0 + hslot(k)); };
    {
        const u32x4 rx = srd(x + (size_t)(64 * tg + 16 * m) * 1024, 16384), rwq = srd(wqkv + (size_t)(16 * m) * 3072 + hg * 128, 512 * 3072);
        const unsigned xv0 = (lane >> 3) * 1024 + (((lane & 7) ^ (lane >> 4)) * 16), xv1 = (xv0 ^ 64) + 8192 - 1024;
        const unsigned wv0 = (lane >> 3) * 3072 + swzc, wv1 = wv0 + 8 * 3072 - 1024;
        auto issue = [&](int i) { const int kc = (i + rot) & 7;
            dma2(rx, kc * 128, xv0, xv1, l0 + ximg(kc) + m * 2048);
#pragma unroll
            for (int sg = 0; sg < 3; ++sg) dma2(rwq, kc * (64 * 3072) + sg * 1024, wv0, wv1, l0 + wimg((i & 3) * 3 + sg) + m * 2048); };
        issue(0); issue(1);
        u64 ep; u32x2 bv[8];
        constexpr int WCNT[8] = {8, 16, 25, 25, 25, 16, 8, 7};
#pragma unroll
        for (int i = 0; i < 8; ++i) {
            if (i == 5) asm volatile("s_waitcnt vmcnt(16)" : "+v"(ep), "+v"(bv[0]), "+v"(bv[1]), "+v"(bv[2]), "+v"(bv[3]), "+v"(bv[4]), "+v"(bv[5]), "+v"(bv[6]), "+v"(bv[7]) :: "memory");
            else WAITV(WCNT[i]);
            BARRIER();
            if (i == 0) { issue(2); issue(3); }
            if (i >= 1 && i <= 4) issue(i + 3);
            if (i == 1) {
                asm volatile("global_load_dwordx2 %0, %1, %2 sc1" : "=v"(ep) : "v"(0u), "s"(ws + F_EPOCH) : "memory");
#pragma unroll
                for (int g = 0; g < 8; ++g) asm volatile("global_load_dwordx2 %0, %1, %2" : "=v"(bv[g]) : "v"(hi * 8), "s"(b1 + (hact ? cp : 0) * 16 + 4 * g) : "memory");
            }
            if (i == 6) {
                dma1(srd(gw, 1024), 0, lo16, l0 + L_B + m * 8192 + 7168); w1dma(0); w1dma(1); w1dma(2);
                if (m == 0 && lane == 0) { *(LAS u32x4*)(lds + L_MISC) = (u32x4){0u, 0u, 0u, 0u}; *(LAS u64*)(lds + L_MISC + 16) = ep + 1; *(LAS u64*)(lds + L_MISC + 24) = 0; }
            }
            if (i == 7) w1dma(3);
        }
        tag = ep + 1;
#pragma unroll
        for (int g = 0; g < 4; ++g)
#pragma unroll
            for (int j = 0; j < 4; ++j) {
                const unsigned u0 = bv[g][j >> 1], u1 = bv[4 + g][j >> 1];
                a00[4 * g + j] = __uint_as_float((j & 1) ? (u0 & 0xffff0000u) : (u0 << 16));
                a10[4 * g + j] = __uint_as_float((j & 1) ? (u1 & 0xffff0000u) : (u1 << 16));
            }
        a01 = a00; a11 = a10;
        BARRIER();
    }
    const u32x4 rws = srd(ws, WS_BYTES);
    {
        __builtin_amdgcn_s_setprio(2);
        if (HGATE == 1) lds_wait(lds + L_MISC + 24, 8);
        if (HGATE == 2) lds_wait(lds + L_MISC + 28, 1);
        if (HGATE == 3) lds_wait(lds + L_MISC, 8);
        const unsigned trn = (8 * hi + ((lane & 15) >> 2)) * 128 + (((lane >> 3) & 1) * 64) + ((lane >> 4) & 1) * 32 + (lane & 3) * 8;
        bf16x8 f0[2], f1[2], xa[2], xc[2];
        float pA = 0.f, pB = 0.f;
        u32x4 gv;
        unsigned xs[4];
#pragma unroll
        for (int j = 0; j < 4; ++j) xs[j] = xfo ^ (((j + m) & 3) * 32);
#pragma unroll
        for (int k = 0; k <= 32; ++k) {
            if ((k & 3) == 0 && k < 32) STAMP(8 + (k >> 2));
            lds_ptr sl = lds + hslot(k);
            unsigned xb = 0;
            if (k < 32) {
                WAITV(k + 4 <= 32 ? 6 : 2 * (31 - k));
                xb = ximg(((k >> 2) + rot) & 7) + xs[k & 3];
            }
            if (k > 0) a00 = MFMA(f0[(k - 1) & 1], xa[(k - 1) & 1], a00);
            if (k < 32) { xa[k & 1] = ldfrag(lds + xb); f0[k & 1] = trfrag(sl + trn, 512); }
            __builtin_amdgcn_sched_barrier(0);
            if (k > 0) a10 = MFMA(f1[(k - 1) & 1], xa[(k - 1) & 1], a10);
            if (k < 32) { f1[k & 1] = trfrag(sl + (trn ^ 64), 512); xc[k & 1] = ldfrag(lds + xb + 4096); }
            __builtin_amdgcn_sched_barrier(0);
            if (k > 0) a01 = MFMA(f0[(k - 1) & 1], xc[(k - 1) & 1], a01);
            __builtin_amdgcn_sched_barrier(0);
            if (k + 4 < 32) w1dma(k + 4);
            if (k > 0) a11 = MFMA(f1[(k - 1) & 1], xc[(k - 1) & 1], a11);
            if ((k & 3) == 1) {
                const u32x4 ua = __builtin_bit_cast(u32x4, xa[(k - 1) & 1]), uc = __builtin_bit_cast(u32x4, xc[(k - 1) & 1]);
#pragma unroll
                for (int j = 0; j < 4; ++j) {
                    const unsigned a_ = ua[j], c_ = uc[j], g_ = gv[j];
                    pA = __builtin_amdgcn_fdot2_f32_bf16(__builtin_bit_cast(bf16x2, a_), __builtin_bit_cast(bf16x2, g_), pA, false);
                    pB = __builtin_amdgcn_fdot2_f32_bf16(__builtin_bit_cast(bf16x2, c_), __builtin_bit_cast(bf16x2, g_), pB, false);
                }
            }
            if (k < 32 && (k & 3) == 0) gv = *(LAS const u32x4*)(ring + 7168 + ((((k >> 2) + rot) & 7) * 64 + 16 * m + 8 * hi) * 2);
            __builtin_amdgcn_sched_barrier(0);
        }
        asm volatile("" : "+v"(a00), "+v"(a01), "+v"(a10), "+v"(a11));
        *(LAS float*)(ring + 6144 + 512 + lane * 4) = pA; *(LAS float*)(ring + 6144 + 768 + lane * 4) = pB;
    }
    STAMP(1);
    lds_arrive(lds + L_MISC + 12, lane);
    lds_wait(lds + L_MISC + 28, 1);
    dma2_dev(rws, WS_K + (head * 64 + 24 + 2 * m) * 1024, lo16, l0 + L_K + (24 + 2 * m) * 1024);
    dma2_dev(rws, WS_V + (head * 64 + 24 + 2 * m) * 1024, lo16, l0 + L_V + (24 + 2 * m) * 1024);
    lds_wait(lds + L_MISC + 12, 4);
#pragma unroll
    for (int j = 0; j < 2; ++j) {
        dma4_dev(rws, WS_K + (head * 64 + 32 + 16 * j + 4 * m) * 1024, lo16, l0 + L_K + (32 + 16 * j + 4 * m) * 1024);
        dma4_dev(rws, WS_V + (head * 64 + 32 + 16 * j + 4 * m) * 1024, lo16, l0 + L_V + (32 + 16 * j + 4 * m) * 1024);
    }
    float zA = 0.f, zB = 0.f;
#pragma unroll
    for (int q = 0; q < 4; ++q) {
        lds_ptr pp = lds + L_B + q * 8192 + 6144 + 512 + (lane & 31) * 4;
        zA += *(LAS const float*)pp + *(LAS const float*)(pp + 128); zB += *(LAS const float*)(pp + 256) + *(LAS const float*)(pp + 384);
    }
    asm volatile("" : "+v"(zA), "+v"(zB));
    {
        const unsigned hd = WS_H + (2 * tg * 112 + 2 * cp) * 1024;
        u32x4 h0, h1, h2, h3;
        if (hact) { h0 = mish8(a00, 0); h1 = mish8(a00, 1); h2 = mish8(a10, 0); h3 = mish8(a10, 1); asm volatile("" : "+v"(h0), "+v"(h1), "+v"(h2), "+v"(h3)); }
        WAITV(0); lds_arrive(lds + L_MISC + 8, lane);
        if (hact) {
            gst_plain_dev(rws, hd, lo16, h0); gst_plain_dev(rws, hd + 1024, lo16, h1); gst_plain_dev(rws, hd + 2048, lo16, h2); gst_plain_dev(rws, hd + 3072, lo16, h3);
            gst_plain_dev(rws, hd + 112 * 1024, lo16, mish8(a01, 0)); gst_plain_dev(rws, hd + 113 * 1024, lo16, mish8(a01, 1));
            gst_plain_dev(rws, hd + 114 * 1024, lo16, mish8(a11, 0)); gst_plain_dev(rws, hd + 115 * 1024, lo16, mish8(a11, 1));
        }
    }
    publish(ws, F_H + (tg * 32 + hg * 4 + m) * FS, tag, lane);
    STAMP(2);
    u32x4 st[16];
    const unsigned stw = swz8 * 8 + swzc;
    const unsigned w2v0 = (lane >> 3) * 1024 + (lane & 7) * 16, w2v1 = w2v0 + 8192, hv1 = lo16 + 112 * 1024;
    const unsigned trp = (4 * hi + ((lane & 15) >> 2)) * 128 + (((lane >> 3) & 1) * 64) + ((lane >> 4) & 1) * 32 + (lane & 3) * 8;
    {
        const u32x4 rw2 = srd(w2 + (size_t)(28 * m) * 16384 + hg * 128, 28 * 16384);
        const unsigned hs = WS_H + (2 * tg * 112 + 28 * m) * 1024;
#pragma unroll
        for (int k = 0; k < 4; ++k) { BLD(st[2 * k], rw2, k * 16384, w2v0); BLD(st[2 * k + 1], rw2, k * 16384, w2v1); }
        poll_tags(ws, F_H + tg * 32 * FS, 32, tag, lane);
        STAMP(3);
#pragma unroll
        for (int k = 0; k < 4; ++k) { BLD_DEV(st[8 + 2 * k], rws, hs + k * 1024, lo16); BLD_DEV(st[9 + 2 * k], rws, hs + k * 1024, hv1); }
        WAITV(0);
        a00 = (f32x16){}; a01 = (f32x16){}; a10 = (f32x16){}; a11 = (f32x16){};
        auto bsteps = [&](int it, auto LAST) {
#pragma unroll
            for (int k = 0; k < 4; ++k) {
                asm volatile("s_waitcnt vmcnt(%4)" : "+v"(st[2 * k]), "+v"(st[2 * k + 1]), "+v"(st[8 + 2 * k]), "+v"(st[9 + 2 * k]) : "i"(LAST.value ? 12 - 4 * k : 12) : "memory");
                lds_ptr sl = ring + (k & 1) * 2048;
                *(LAS u32x4*)(sl + stw) = st[2 * k]; *(LAS u32x4*)(sl + 1024 + stw) = st[2 * k + 1];
                const bf16x8 f0 = trfrag(sl + trp, 1024), f1 = trfrag(sl + (trp ^ 64), 1024);
                const bf16x8 xa = asfrag(st[8 + 2 * k]), xc = asfrag(st[9 + 2 * k]);
                a00 = MFMA(f0, xa, a00); PACE(); a01 = MFMA(f0, xc, a01); PACE(); a10 = MFMA(f1, xa, a10); PACE(); a11 = MFMA(f1, xc, a11); PACE();
                if (!LAST.value) { const int kn = 4 * (it + 1) + k;
                    BLD(st[2 * k], rw2, kn * 16384, w2v0); BLD(st[2 * k + 1], rw2, kn * 16384, w2v1);
                    BLD_DEV(st[8 + 2 * k], rws, hs + kn * 1024, lo16); BLD_DEV(st[9 + 2 * k], rws, hs + kn * 1024, hv1); }
            } };
#pragma unroll 1
        for (int it = 0; it < 6; ++it) bsteps(it, std::false_type{});
        bsteps(6, std::true_type{});
        asm volatile("" : "+v"(a00), "+v"(a01), "+v"(a10), "+v"(a11));
    }
    STAMP(4);
    zA = __builtin_fmaxf(zA, -30.f); zB = __builtin_fmaxf(zB, -30.f);
    const float rhoA = __builtin_amdgcn_exp2f(-zA * LOG2E), rhoB = __builtin_amdgcn_exp2f(-zB * LOG2E);
    bf16x8 wf[10];
    {
        const u32x4 rwo = srd(wo + (size_t)(8 * m) * 16384 + hg * 128, 8 * 16384);
#pragma unroll
        for (int k = 0; k < 8; ++k) { BLD(st[2 * k], rwo, k * 16384, w2v0); BLD(st[2 * k + 1], rwo, k * 16384, w2v1); }
#pragma unroll
        for (int i = 0; i < 16; ++i) { a00[i] *= rhoA; a10[i] *= rhoA; a01[i] *= rhoB; a11[i] *= rhoB; }
        asm volatile("" : "+v"(a00), "+v"(a01), "+v"(a10), "+v"(a11));
#pragma unroll
        for (int k = 0; k < 8; ++k) {
            asm volatile("s_waitcnt vmcnt(%2)" : "+v"(st[2 * k]), "+v"(st[2 * k + 1]) : "i"(14 - 2 * k) : "memory");
            lds_ptr sl = ring + (k < 5 ? (k & 1) : (k - 5)) * 2048;
            *(LAS u32x4*)(sl + stw) = st[2 * k]; *(LAS u32x4*)(sl + 1024 + stw) = st[2 * k + 1];
            if (k < 5) { wf[2 * k] = trfrag(sl + trp, 1024); wf[2 * k + 1] = trfrag(sl + (trp ^ 64), 1024); }
        }
    }
#pragma unroll
    for (int tt = 0; tt < 2; ++tt) {
        poll_tags(ws, F_O + ((2 * tg + tt) * 32 + 8 * m) * FS, 8, tag, lane);
        const unsigned os = WS_O + ((2 * tg + tt) * 32 + 8 * m) * 1024;
#pragma unroll
        for (int k = 0; k < 8; ++k) BLD_DEV(st[k], rws, os + k * 1024, lo16);
#pragma unroll
        for (int k = 0; k < 8; ++k) {
            bf16x8 f0, f1;
            if (k < 5) { f0 = wf[2 * k]; f1 = wf[2 * k + 1]; } else { f0 = trfrag(ring + (k - 5) * 2048 + trp, 1024); f1 = trfrag(ring + (k - 5) * 2048 + (trp ^ 64), 1024); }
            asm volatile("s_waitcnt vmcnt(%1)" : "+v"(st[k]) : "i"(7 - k) : "memory");
            if (tt == 0) { a00 = MFMA(f0, asfrag(st[k]), a00); a10 = MFMA(f1, asfrag(st[k]), a10); }
            else         { a01 = MFMA(f0, asfrag(st[k]), a01); a11 = MFMA(f1, asfrag(st[k]), a11); }
        }
        STAMP(5 + tt);
    }
    BARRIER();
    if (b == 0 && tid == 512) __hip_atomic_store((GAS u64*)(ws + F_EPOCH), tag, __ATOMIC_RELAXED, __HIP_MEMORY_SCOPE_AGENT);
    auto put = [&](const f32x16& a, int ti) {
        if (ti != m) {
#pragma unroll
            for (int q = 0; q < 4; ++q) *(LAS f32x4*)(lds + ((ti * 4 + m) * 4 + q) * 1024 + lo16) = (f32x4){a[4 * q], a[4 * q + 1], a[4 * q + 2], a[4 * q + 3]};
        } };
    put(a00, 0); put(a01, 1); put(a10, 2); put(a11, 3);
    BARRIER();
    f32x16 acc = m == 0 ? a00 : m == 1 ? a01 : m == 2 ? a10 : a11;
#pragma unroll
    for (int o = 1; o < 4; ++o) {
        const int src = (m + o) & 3;
#pragma unroll
        for (int q = 0; q < 4; ++q) { const f32x4 v = *(LAS const f32x4*)(lds + ((m * 4 + src) * 4 + q) * 1024 + lo16); acc[4 * q] += v[0]; acc[4 * q + 1] += v[1]; acc[4 * q + 2] += v[2]; acc[4 * q + 3] += v[3]; }
    }
    const float g = __builtin_amdgcn_rcpf(1.f + ((m & 1) ? rhoB : rhoA));
    char* op = out + (size_t)(64 * tg + (m & 1) * 32 + (lane & 31)) * 1024 + hg * 128 + (m >> 1) * 64 + hi * 16;
#pragma unroll
    for (int p = 0; p < 2; ++p) {
        const unsigned a0 = cvtpk(acc[8 * p] * g, acc[8 * p + 1] * g), a1 = cvtpk(acc[8 * p + 2] * g, acc[8 * p + 3] * g);
        const unsigned b0 = cvtpk(acc[8 * p + 4] * g, acc[8 * p + 5] * g), b1v = cvtpk(acc[8 * p + 6] * g, acc[8 * p + 7] * g);
        auto r0 = __builtin_amdgcn_permlane32_swap(a0, b0, false, false);
        auto r1 = __builtin_amdgcn_permlane32_swap(a1, b1v, false, false);
        *(u32x4*)(op + p * 32) = (u32x4){r0[0], r1[0], r0[1], r1[1]};
    }
    STAMP(7);
    STAMP_FLUSH();
}

extern "C" void kernel_launch(void* const* d_in, const int* in_sizes, int n_in, void* d_out, int out_size, void* d_ws, size_t ws_size, hipStream_t stream) {
    (void)hipFuncSetAttribute((const void*)fused_kernel, hipFuncAttributeMaxDynamicSharedMemorySize, LDS_BYTES);
    fused_kernel<<<256, 768, LDS_BYTES, stream>>>((const char*)d_in[0], (const char*)d_in[1], (const char*)d_in[2], (const char*)d_in[4], (const unsigned*)d_in[5],
                                                  (const char*)d_in[6], (const char*)d_in[7], (char*)d_ws, (char*)d_out);
}
```

```cpp
#include <hip/hip_runtime.h>
#include <hip/hip_bf16.h>
#include <stdint.h>
#include <type_traits>

using bf16x8 = __attribute__((ext_vector_type(8))) short;
using s16x4  = __attribute__((ext_vector_type(4))) short;
using f32x16 = __attribute__((ext_vector_type(16))) float;
using f32x4  = __attribute__((ext_vector_type(4))) float;
using f32x2  = __attribute__((ext_vector_type(2))) float;
using u32x4  = __attribute__((ext_vector_type(4))) unsigned;
using u32x2  = __attribute__((ext_vector_type(2))) unsigned;
typedef __bf16 bf16x2 __attribute__((ext_vector_type(2)));
typedef short v4i16_t __attribute__((ext_vector_type(4)));
typedef unsigned long long u64;
#define DI __device__ __forceinline__
#define LAS __attribute__((address_space(3)))
#define GAS __attribute__((address_space(1)))
#define MFMA(a, b, c) __builtin_amdgcn_mfma_f32_32x32x16_bf16((a), (b), (c), 0, 0, 0)
typedef LAS char* lds_ptr;

constexpr unsigned WS_Q = 0, WS_K = 2u << 20, WS_V = 4u << 20, WS_O = 6u << 20, WS_H = 8u << 20, WS_F = 15u << 20;
constexpr unsigned WS_BYTES = 17u << 20;
#define FS 128
constexpr unsigned F_EPOCH = WS_F, F_QKV = WS_F + 4096, F_H = F_QKV + 256 * FS, F_O = F_H + 1024 * FS;
constexpr unsigned L_K = 0, L_V = 65536, L_B = 131072, L_MISC = L_B + 6144, LDS_BYTES = 163840;
#define FIRST_SLEEP 16
#define HGATE 0
#define TAILPRIO 2
#define RETRY_SLEEP 2
#define PRIO_DROP 7
constexpr unsigned SPIN_CAP = 1u << 16;
constexpr float LOG2E = 1.4426950408889634f;
constexpr float QSCALE = 0.25f * LOG2E;

DI unsigned cvtpk(float lo, float hi) { return __builtin_bit_cast(unsigned, __builtin_convertvector((f32x2){lo, hi}, bf16x2)); }
DI u32x4 pack8(const f32x16& a, int s) { return (u32x4){cvtpk(a[8*s], a[8*s+1]), cvtpk(a[8*s+2], a[8*s+3]), cvtpk(a[8*s+4], a[8*s+5]), cvtpk(a[8*s+6], a[8*s+7])}; }
DI u32x4 srd(const void* p, unsigned bytes) { const u64 a = (u64)p; return (u32x4){(unsigned)a, (unsigned)(a >> 32) & 0xffffu, bytes, 0x00020000u}; }
DI void dma2(u32x4 rs, unsigned so, unsigned v0, unsigned v1, unsigned dst) {
    asm volatile("s_mov_b32 m0, %4\n\ts_nop 0\n\tbuffer_load_dwordx4 %0, %2, %3 offen lds\n\tbuffer_load_dwordx4 %1, %2, %3 offen offset:1024 lds"
                 :: "v"(v0), "v"(v1), "s"(rs), "s"(so), "s"(dst) : "memory"); }
DI void dma1(u32x4 rs, unsigned so, unsigned v0, unsigned dst) {
    asm volatile("s_mov_b32 m0, %3\n\ts_nop 0\n\tbuffer_load_dwordx4 %0, %1, %2 offen lds" :: "v"(v0), "s"(rs), "s"(so), "s"(dst) : "memory"); }
DI void dma4_dev(u32x4 rs, unsigned so, unsigned v0, unsigned dst) {
    asm volatile("s_mov_b32 m0, %3\n\ts_nop 0\n\tbuffer_load_dwordx4 %0, %1, %2 offen sc1 lds\n\tbuffer_load_dwordx4 %0, %1, %2 offen offset:1024 sc1 lds\n\t"
                 "buffer_load_dwordx4 %0, %1, %2 offen offset:2048 sc1 lds\n\tbuffer_load_dwordx4 %0, %1, %2 offen offset:3072 sc1 lds"
                 :: "v"(v0), "s"(rs), "s"(so), "s"(dst) : "memory"); }
DI void dma1_dev(u32x4 rs, unsigned so, unsigned v0, unsigned dst) {
    asm volatile("s_mov_b32 m0, %3\n\ts_nop 0\n\tbuffer_load_dwordx4 %0, %1, %2 offen sc1 lds" :: "v"(v0), "s"(rs), "s"(so), "s"(dst) : "memory"); }
DI void dma2_dev(u32x4 rs, unsigned so, unsigned v0, unsigned dst) {
    asm volatile("s_mov_b32 m0, %3\n\ts_nop 0\n\tbuffer_load_dwordx4 %0, %1, %2 offen sc1 lds\n\tbuffer_load_dwordx4 %0, %1, %2 offen offset:1024 sc1 lds"
                 :: "v"(v0), "s"(rs), "s"(so), "s"(dst) : "memory"); }
DI void BLD(u32x4& dst, u32x4 rs, unsigned so, unsigned vo) { asm volatile("buffer_load_dwordx4 %0, %1, %2, %3 offen" : "=v"(dst) : "v"(vo), "s"(rs), "s"(so) : "memory"); }
DI void BLD_DEV(u32x4& dst, u32x4 rs, unsigned so, unsigned vo) { asm volatile("buffer_load_dwordx4 %0, %1, %2, %3 offen sc1" : "=v"(dst) : "v"(vo), "s"(rs), "s"(so) : "memory"); }
DI void gst_tagged(u32x4 rs, unsigned so, unsigned vo, u32x4 v, unsigned tag) {
    const u32x4 a = {v[0], tag, v[1], tag}, b = {v[2], tag, v[3], tag};
    asm volatile("buffer_store_dwordx4 %0, %2, %3, %4 offen sc1\n\tbuffer_store_dwordx4 %1, %2, %3, %4 offen offset:1024 sc1\n\ts_nop 1" :: "v"(a), "v"(b), "v"(vo), "s"(rs), "s"(so) : "memory"); }
DI void gst_plain_dev(u32x4 rs, unsigned so, unsigned vo, u32x4 v) {
    asm volatile("buffer_store_dwordx4 %0, %1, %2, %3 offen sc1\n\ts_nop 1" :: "v"(v), "v"(vo), "s"(rs), "s"(so) : "memory"); }
DI void ld_tagged(u32x4& a, u32x4& b, u32x4 rs, unsigned so, unsigned vo) {
    asm volatile("buffer_load_dwordx4 %0, %2, %3, %4 offen sc1\n\tbuffer_load_dwordx4 %1, %2, %3, %4 offen offset:1024 sc1" : "=&v"(a), "=&v"(b) : "v"(vo), "s"(rs), "s"(so) : "memory"); }
DI bool tags_ok(const u32x4& a, const u32x4& b, unsigned tag) { return __all(((a[1] ^ tag) | (a[3] ^ tag) | (b[1] ^ tag) | (b[3] ^ tag)) == 0u); }
DI void GLD(u32x4& dst, const void* sbase, unsigned voff) { asm volatile("global_load_dwordx4 %0, %1, %2" : "=v"(dst) : "v"(voff), "s"(sbase) : "memory"); }
DI void GLD_DEV(u32x4& dst, const void* sbase, unsigned voff) { asm volatile("global_load_dwordx4 %0, %1, %2 sc1" : "=v"(dst) : "v"(voff), "s"(sbase) : "memory"); }
DI void gst_dev(const void* sbase, unsigned voff, u32x4 v) { asm volatile("global_store_dwordx4 %0, %1, %2 sc1\n\ts_nop 1" :: "v"(voff), "v"(v), "s"(sbase) : "memory"); }
DI s16x4 trd(lds_ptr p) { return __builtin_bit_cast(s16x4, __builtin_amdgcn_ds_read_tr16_b64_v4i16((LAS v4i16_t*)p)); }
DI bf16x8 trfrag(lds_ptr p, int second) { s16x4 a = trd(p), b = trd(p + second); return __builtin_shufflevector(a, b, 0, 1, 2, 3, 4, 5, 6, 7); }
DI bf16x8 ldfrag(lds_ptr p) { return *(LAS const bf16x8*)p; }
DI bf16x8 asfrag(u32x4 v) { return __builtin_bit_cast(bf16x8, v); }
#define PACE_N 0
#define PACE() do { __builtin_amdgcn_sched_barrier(0); if (PACE_N) __builtin_amdgcn_s_sleep(PACE_N); __builtin_amdgcn_sched_barrier(0); } while (0)
#define WAITV(n) asm volatile("s_waitcnt vmcnt(%0)" :: "i"(n) : "memory")
#define BARRIER() do { asm volatile("" ::: "memory"); __builtin_amdgcn_s_waitcnt(0xC07F); __builtin_amdgcn_s_barrier(); asm volatile("" ::: "memory"); } while (0)
DI unsigned mish2(float h0, float h1) {
    const f32x2 h = {h0, h1}, e = h * LOG2E;
    const f32x2 u = {__builtin_amdgcn_exp2f(e[0]), __builtin_amdgcn_exp2f(e[1])};
    const f32x2 two = {2.f, 2.f}, d = __builtin_elementwise_fma(u, u + two, two);
    const f32x2 r = {__builtin_amdgcn_rcpf(d[0]), __builtin_amdgcn_rcpf(d[1])};
    const f32x2 y = h * __builtin_elementwise_fma(r, (f32x2){-2.f, -2.f}, (f32x2){1.f, 1.f});
    return cvtpk(y[0], y[1]);
}
DI u32x4 mish8(const f32x16& a, int s) { return (u32x4){mish2(a[8*s], a[8*s+1]), mish2(a[8*s+2], a[8*s+3]), mish2(a[8*s+4], a[8*s+5]), mish2(a[8*s+6], a[8*s+7])}; }
DI float halfmax(float m) { auto rr = __builtin_amdgcn_permlane32_swap(__float_as_uint(m), __float_as_uint(m), false, false); return __builtin_fmaxf(__uint_as_float(rr[0]), __uint_as_float(rr[1])); }
DI float max16(const f32x16& p) {
    float a = __builtin_fmaxf(__builtin_fmaxf(p[0], p[1]), p[2]), b = __builtin_fmaxf(__builtin_fmaxf(p[3], p[4]), p[5]);
    a = __builtin_fmaxf(__builtin_fmaxf(a, p[6]), p[7]); b = __builtin_fmaxf(__builtin_fmaxf(b, p[8]), p[9]);
    a = __builtin_fmaxf(__builtin_fmaxf(a, p[10]), p[11]); b = __builtin_fmaxf(__builtin_fmaxf(b, p[12]), p[13]);
    a = __builtin_fmaxf(__builtin_fmaxf(a, p[14]), p[15]); return __builtin_fmaxf(a, b); }
DI void publish(char* ws, unsigned off, u64 tag, int lane) {
    asm volatile("s_waitcnt vmcnt(0)" ::: "memory");
    if (lane == 0) __hip_atomic_store((GAS u64*)(ws + off), tag, __ATOMIC_RELAXED, __HIP_MEMORY_SCOPE_AGENT);
}
DI void poll_tags(char* ws, unsigned off, int n, u64 tag, int lane) {
    GAS u64* p = (GAS u64*)(ws + off + (lane < n ? lane : 0) * FS);
#pragma unroll 1
    for (unsigned s = 0; s < SPIN_CAP; ++s) {
        const u64 v = __hip_atomic_load(p, __ATOMIC_RELAXED, __HIP_MEMORY_SCOPE_AGENT);
        if (__all(v == tag)) break;
        __builtin_amdgcn_s_sleep(2);
    }
    asm volatile("" ::: "memory");
}
DI void poll_at(char* p, u64 tag) {
#pragma unroll 1
    for (unsigned s = 0; s < SPIN_CAP; ++s) {
        const u64 v = __hip_atomic_load((GAS u64*)p, __ATOMIC_RELAXED, __HIP_MEMORY_SCOPE_AGENT);
        if (__all(v == tag)) break;
        __builtin_amdgcn_s_sleep(4);
    }
    asm volatile("" ::: "memory");
}
DI void lds_wait_slow(lds_ptr c, unsigned n) {
#pragma unroll 1
    for (unsigned s = 0; s < SPIN_CAP * 8; ++s) {
        const unsigned v = __builtin_amdgcn_readfirstlane(__hip_atomic_load((LAS unsigned*)c, __ATOMIC_RELAXED, __HIP_MEMORY_SCOPE_WORKGROUP));
        if (v >= n) break;
        __builtin_amdgcn_s_sleep(3);
    }
    asm volatile("" ::: "memory");
}
DI void lds_arrive(lds_ptr c, int lane) {
    asm volatile("s_waitcnt lgkmcnt(0)" ::: "memory");
    if (lane == 0) __hip_atomic_fetch_add((LAS unsigned*)c, 1u, __ATOMIC_RELAXED, __HIP_MEMORY_SCOPE_WORKGROUP);
}
DI void lds_wait(lds_ptr c, unsigned n) {
#pragma unroll 1
    for (unsigned s = 0; s < SPIN_CAP * 8; ++s) {
        const unsigned v = __builtin_amdgcn_readfirstlane(__hip_atomic_load((LAS unsigned*)c, __ATOMIC_RELAXED, __HIP_MEMORY_SCOPE_WORKGROUP));
        if (v >= n) break;
        __builtin_amdgcn_s_sleep(1);
    }
    asm volatile("" ::: "memory");
}
#define STAMP(i)
#define CSTAMP(i)
#define STAMP_DECL()
#define STAMP_FLUSH()
DI unsigned ximg(int kc) { return 32768u + (kc & 3) * 8192u + (kc >> 2) * 65536u; }
constexpr unsigned wimg(int n) { return n < 4 ? L_B + n * 8192u : n < 8 ? L_K + (n - 4) * 8192u : L_V + (n - 8) * 8192u; }

__global__ __launch_bounds__(768, 3) void fused_kernel(const char* __restrict__ x, const char* __restrict__ wqkv, const char* __restrict__ wo,
                                                       const char* __restrict__ w1, const unsigned* __restrict__ b1, const char* __restrict__ w2,
                                                       const char* __restrict__ gw, char* __restrict__ ws, char* __restrict__ out) {
    extern __shared__ __attribute__((aligned(16))) char lds_raw[];
    lds_ptr lds = (lds_ptr)lds_raw;
    const unsigned l0 = (unsigned)(uintptr_t)lds_raw;
    const int tid = threadIdx.x, lane = tid & 63, w = __builtin_amdgcn_readfirstlane(tid >> 6), hi = lane >> 5;
    const int b = blockIdx.x, xcd = b & 7, bi = b >> 3;
    const int tg = (xcd >> 1) * 8 + (bi & 7), hg = (xcd & 1) * 4 + (bi >> 3);
    const int rot = ((bi & 7) + 2 * (bi >> 3)) & 7;
    const int head = xcd * 4 + (bi & 3);
    const unsigned lo16 = lane * 16;
    const unsigned xfo = (lane & 31) * 128 + ((hi ^ ((lane >> 1) & 7)) * 16);
    STAMP_DECL();
    STAMP(0);
    if (w < 8) {
        const u32x4 rws = srd(ws, WS_BYTES);
        const int ct = (w < 4) ? (w & 1) : ((w >> 1) & 1);
        const unsigned wfo = (8 * hi + ((lane & 15) >> 2)) * 128 + ((ct ^ ((lane >> 3) & 1)) * 64) + ((lane >> 4) & 1) * 32 + (lane & 3) * 8;
        f32x16 acc0 = {}, acc1 = {};
        u64 tag;
        if (w < 4) {
            const unsigned sgo = (w >> 1) * 8192u;
#pragma unroll
            for (int i = 0; i < 8; ++i) {
                BARRIER();
                if (i == 0 || i == 7) STAMP(8 + i);
                const unsigned xb = ximg((i + rot) & 7) + xfo;
                const unsigned wb = ((i & 3) == 1 ? (w < 2 ? wimg(3) : wimg(4)) : wimg((i & 3) * 3) + sgo) + wfo;
                bf16x8 wf[4], xa[4], xc[4];
#pragma unroll
                for (int ks = 0; ks < 4; ++ks) { wf[ks] = trfrag(lds + wb + ks * 2048, 512); xa[ks] = ldfrag(lds + (xb ^ (ks * 32))); xc[ks] = ldfrag(lds + (xb ^ (ks * 32)) + 4096); }
                if (i == 7) { tag = *(LAS const u64*)(lds + L_MISC + 16); BARRIER(); }
                __builtin_amdgcn_sched_barrier(0);
#pragma unroll
                for (int ks = 0; ks < 4; ++ks) { acc0 = MFMA(wf[ks], xa[ks], acc0); acc1 = MFMA(wf[ks], xc[ks], acc1); }
            }
            const unsigned fr = (4 * hg + 2 * ct) * 64 + 2 * tg;
            if (w < 2) {
#pragma unroll
                for (int i = 0; i < 16; ++i) { acc0[i] *= QSCALE; acc1[i] *= QSCALE; }
            }
            const unsigned so = (w < 2 ? WS_Q : WS_K) + fr * 1024;
            gst_plain_dev(rws, so, lo16, pack8(acc0, 0)); gst_plain_dev(rws, so + 1024, lo16, pack8(acc1, 0));
            gst_plain_dev(rws, so + 65536, lo16, pack8(acc0, 1)); gst_plain_dev(rws, so + 65536 + 1024, lo16, pack8(acc1, 1));
        } else {
            const int tt = w & 1;
#pragma unroll
            for (int i = 0; i < 8; ++i) {
                BARRIER();
                if (i == 0 || i == 7) STAMP(8 + i);
                const unsigned xb = ximg((i + rot) & 7) + xfo + tt * 4096;
                bf16x8 wf[4], xa[4];
#pragma unroll
                for (int ks = 0; ks < 4; ++ks) { wf[ks] = trfrag(lds + wimg((i & 3) * 3 + 2) + wfo + ks * 2048, 512); xa[ks] = ldfrag(lds + (xb ^ (ks * 32))); }
                if (i == 7) { tag = *(LAS const u64*)(lds + L_MISC + 16); BARRIER(); }
                __builtin_amdgcn_sched_barrier(0);
#pragma unroll
                for (int ks = 0; ks < 4; ++ks) acc0 = MFMA(xa[ks], wf[ks], acc0);
            }
            const unsigned vo = hi * 256 + (lane & 15) * 16, h2 = (lane >> 4) & 1;
            const unsigned so = WS_V + ((4 * hg + 2 * ct) * 64 + 2 * tg + tt) * 1024;
            gst_plain_dev(rws, so, vo + h2 * 65536, pack8(acc0, 0)); gst_plain_dev(rws, so, vo + h2 * 65536 + 512, pack8(acc0, 1));
        }
        STAMP(1);
        WAITV(0);
        STAMP(6);
        if (__builtin_amdgcn_readfirstlane(lane == 0 ? __hip_atomic_fetch_add((LAS unsigned*)(lds + L_MISC + 24), 1u, __ATOMIC_RELAXED, __HIP_MEMORY_SCOPE_WORKGROUP) : 0u) == 7u) {
            if (lane == 0) __hip_atomic_store((GAS u64*)(ws + F_QKV + (hg * 32 + tg) * FS), tag, __ATOMIC_RELAXED, __HIP_MEMORY_SCOPE_AGENT);
            poll_tags(ws, F_QKV + (head >> 2) * 32 * FS, 32, tag, lane);
            if (lane == 0) __hip_atomic_store((LAS unsigned*)(lds + L_MISC + 28), 1u, __ATOMIC_RELAXED, __HIP_MEMORY_SCOPE_WORKGROUP);
        } else lds_wait(lds + L_MISC + 28, 1);
        STAMP(2);
        const int tile = 8 * (bi >> 2) + (w < 4 ? 2 * w : 2 * (w - 4) + 1);
        u32x4 qr;
        BLD_DEV(qr, rws, WS_Q + (head * 64 + tile) * 1024, lo16);
#pragma unroll
        for (int j = 0; j < 3; ++j) { dma1_dev(rws, WS_K + (head * 64 + j * 8 + w) * 1024, lo16, l0 + L_K + (j * 8 + w) * 1024); dma1_dev(rws, WS_V + (head * 64 + j * 8 + w) * 1024, lo16, l0 + L_V + (j * 8 + w) * 1024); }
        asm volatile("s_waitcnt vmcnt(4)" : "+v"(qr) :: "memory");
        const bf16x8 qf = asfrag(qr);
        lds_arrive(lds + L_MISC, lane); lds_wait(lds + L_MISC, 8);
        STAMP(3); CSTAMP(10);
        if (w >= 4) __builtin_amdgcn_s_setprio(1);
        const f32x16 zero = {};
        const float nm = -halfmax(max16(MFMA(ldfrag(lds + L_K + lo16), qf, zero)));
        float o[9];
        {
            unsigned ka = l0 + L_K + lo16, va = l0 + L_V + hi * 256 + (lane & 15) * 16; const unsigned cnt_addr = l0 + L_MISC;
            unsigned s_it, s_sp, s_st;
            const unsigned onev = ((lane & 27) == 16) ? 0x3f803f80u : 0u;
    asm volatile(
        "ds_read_b128 v[136:139], %[ka]\n\t"
        "ds_read_b128 v[140:143], %[ka] offset:1024\n\t"
        "v_mov_b32 v144, %[one]\n\t"
        "v_mov_b32 v148, %[one]\n\t"
        "v_mov_b32 v145, %[one]\n\t"
        "v_mov_b32 v149, %[one]\n\t"
        "v_mov_b32 v146, %[one]\n\t"
        "v_mov_b32 v150, %[one]\n\t"
        "v_mov_b32 v147, %[one]\n\t"
        "v_mov_b32 v151, %[one]\n\t"
        "v_mov_b32 v152, %[one]\n\t"
        "v_mov_b32 v156, %[one]\n\t"
        "v_mov_b32 v153, %[one]\n\t"
        "v_mov_b32 v157, %[one]\n\t"
        "v_mov_b32 v154, %[one]\n\t"
        "v_mov_b32 v158, %[one]\n\t"
        "v_mov_b32 v155, %[one]\n\t"
        "v_mov_b32 v159, %[one]\n\t"
        "s_mov_b64 exec, %[vm]\n\t"
        "ds_read_b128 v[144:147], %[va] offset:0\n\t"
        "ds_read_b128 v[148:151], %[va] offset:512\n\t"
        "s_mov_b64 exec, -1\n\t"
        "v_mov_b32 v112, %[nm]\n\t"
        "v_mov_b32 v113, %[nm]\n\t"
        "v_mov_b32 v114, %[nm]\n\t"
        "v_mov_b32 v115, %[nm]\n\t"
        "v_mov_b32 v116, %[nm]\n\t"
        "v_mov_b32 v117, %[nm]\n\t"
        "v_mov_b32 v118, %[nm]\n\t"
        "v_mov_b32 v119, %[nm]\n\t"
        "v_mov_b32 v120, %[nm]\n\t"
        "v_mov_b32 v121, %[nm]\n\t"
        "v_mov_b32 v122, %[nm]\n\t"
        "v_mov_b32 v123, %[nm]\n\t"
        "v_mov_b32 v124, %[nm]\n\t"
        "v_mov_b32 v125, %[nm]\n\t"
        "v_mov_b32 v126, %[nm]\n\t"
        "v_mov_b32 v127, %[nm]\n\t"
        "v_mov_b32 v96, 0\n\t"
        "v_mov_b32 v97, 0\n\t"
        "v_mov_b32 v98, 0\n\t"
        "v_mov_b32 v99, 0\n\t"
        "v_mov_b32 v100, 0\n\t"
        "v_mov_b32 v101, 0\n\t"
        "v_mov_b32 v102, 0\n\t"
        "v_mov_b32 v103, 0\n\t"
        "v_mov_b32 v104, 0\n\t"
        "v_mov_b32 v105, 0\n\t"
        "v_mov_b32 v106, 0\n\t"
        "v_mov_b32 v107, 0\n\t"
        "v_mov_b32 v108, 0\n\t"
        "v_mov_b32 v109, 0\n\t"
        "v_mov_b32 v110, 0\n\t"
        "v_mov_b32 v111, 0\n\t"
        "v_mov_b32 v132, 0\n\t"
        "v_mov_b32 v133, 0\n\t"
        "v_mov_b32 v134, 0\n\t"
        "v_mov_b32 v135, 0\n\t"
        "s_mov_b32 %[it], 0\n\t"
        "s_waitcnt lgkmcnt(3)\n\t"
        "v_mfma_f32_32x32x16_bf16 v[64:79], v[136:139], %[q], v[112:127]\n\t"
        "1:\n\t"
        "s_cmp_eq_u32 %[it], 1\n\t"
        "s_cbranch_scc1 3f\n\t"
        "s_cmp_eq_u32 %[it], 5\n\t"
        "s_cbranch_scc1 4f\n\t"
        "s_cmp_lg_u32 %[it], %[pd]\n\t"
        "s_cbranch_scc1 2f\n\t"
        "s_setprio 0\n\t"
        "2:\n\t"
        "s_waitcnt lgkmcnt(2)\n\t"
        "v_mfma_f32_32x32x16_bf16 v[80:95], v[140:143], %[q], v[112:127]\n\t"
        "ds_read_b128 v[136:139], %[ka] offset:2048\n\t"
        "v_exp_f32 v64, v64\n\t"
        "v_exp_f32 v65, v65\n\t"
        "v_exp_f32 v66, v66\n\t"
        "v_exp_f32 v67, v67\n\t"
        "v_exp_f32 v68, v68\n\t"
        "v_exp_f32 v69, v69\n\t"
        "v_mfma_f32_32x32x16_bf16 v[96:111], v[156:159], v[132:135], v[96:111]\n\t"
        "s_mov_b64 exec, %[vm]\n\t"
        "ds_read_b128 v[152:155], %[va] offset:1024\n\t"
        "ds_read_b128 v[156:159], %[va] offset:1536\n\t"
        "s_mov_b64 exec, -1\n\t"
        "v_exp_f32 v70, v70\n\t"
        "v_exp_f32 v71, v71\n\t"
        "v_cvt_pk_bf16_f32 v128, v64, v65\n\t"
        "v_cvt_pk_bf16_f32 v129, v66, v67\n\t"
        "v_cvt_pk_bf16_f32 v130, v68, v69\n\t"
        "v_cvt_pk_bf16_f32 v131, v70, v71\n\t"
        "v_exp_f32 v72, v72\n\t"
        "v_exp_f32 v73, v73\n\t"
        "v_exp_f32 v74, v74\n\t"
        "s_waitcnt lgkmcnt(3)\n\t"
        "v_mfma_f32_32x32x16_bf16 v[96:111], v[144:147], v[128:131], v[96:111]\n\t"
        "v_exp_f32 v75, v75\n\t"
        "v_exp_f32 v76, v76\n\t"
        "v_exp_f32 v77, v77\n\t"
        "v_exp_f32 v78, v78\n\t"
        "v_exp_f32 v79, v79\n\t"
        "v_cvt_pk_bf16_f32 v132, v72, v73\n\t"
        "v_cvt_pk_bf16_f32 v133, v74, v75\n\t"
        "v_cvt_pk_bf16_f32 v134, v76, v77\n\t"
        "v_cvt_pk_bf16_f32 v135, v78, v79\n\t"
        "s_waitcnt lgkmcnt(2)\n\t"
        "v_mfma_f32_32x32x16_bf16 v[64:79], v[136:139], %[q], v[112:127]\n\t"
        "ds_read_b128 v[140:143], %[ka] offset:3072\n\t"
        "v_exp_f32 v80, v80\n\t"
        "v_exp_f32 v81, v81\n\t"
        "v_exp_f32 v82, v82\n\t"
        "v_exp_f32 v83, v83\n\t"
        "v_exp_f32 v84, v84\n\t"
        "v_exp_f32 v85, v85\n\t"
        "v_mfma_f32_32x32x16_bf16 v[96:111], v[148:151], v[132:135], v[96:111]\n\t"
        "s_mov_b64 exec, %[vm]\n\t"
        "ds_read_b128 v[144:147], %[va] offset:2048\n\t"
        "ds_read_b128 v[148:151], %[va] offset:2560\n\t"
        "s_mov_b64 exec, -1\n\t"
        "v_exp_f32 v86, v86\n\t"
        "v_exp_f32 v87, v87\n\t"
        "v_cvt_pk_bf16_f32 v128, v80, v81\n\t"
        "v_cvt_pk_bf16_f32 v129, v82, v83\n\t"
        "v_cvt_pk_bf16_f32 v130, v84, v85\n\t"
        "v_cvt_pk_bf16_f32 v131, v86, v87\n\t"
        "v_exp_f32 v88, v88\n\t"
        "v_exp_f32 v89, v89\n\t"
        "v_exp_f32 v90, v90\n\t"
        "s_waitcnt lgkmcnt(3)\n\t"
        "v_mfma_f32_32x32x16_bf16 v[96:111], v[152:155], v[128:131], v[96:111]\n\t"
        "v_exp_f32 v91, v91\n\t"
        "v_exp_f32 v92, v92\n\t"
        "v_exp_f32 v93, v93\n\t"
        "v_exp_f32 v94, v94\n\t"
        "v_exp_f32 v95, v95\n\t"
        "v_cvt_pk_bf16_f32 v132, v88, v89\n\t"
        "v_cvt_pk_bf16_f32 v133, v90, v91\n\t"
        "v_cvt_pk_bf16_f32 v134, v92, v93\n\t"
        "v_cvt_pk_bf16_f32 v135, v94, v95\n\t"
        "s_waitcnt lgkmcnt(2)\n\t"
        "v_mfma_f32_32x32x16_bf16 v[80:95], v[140:143], %[q], v[112:127]\n\t"
        "ds_read_b128 v[136:139], %[ka] offset:4096\n\t"
        "v_exp_f32 v64, v64\n\t"
        "v_exp_f32 v65, v65\n\t"
        "v_exp_f32 v66, v66\n\t"
        "v_exp_f32 v67, v67\n\t"
        "v_exp_f32 v68, v68\n\t"
        "v_exp_f32 v69, v69\n\t"
        "v_mfma_f32_32x32x16_bf16 v[96:111], v[156:159], v[132:135], v[96:111]\n\t"
        "s_mov_b64 exec, %[vm]\n\t"
        "ds_read_b128 v[152:155], %[va] offset:3072\n\t"
        "ds_read_b128 v[156:159], %[va] offset:3584\n\t"
        "s_mov_b64 exec, -1\n\t"
        "v_exp_f32 v70, v70\n\t"
        "v_exp_f32 v71, v71\n\t"
        "v_cvt_pk_bf16_f32 v128, v64, v65\n\t"
        "v_cvt_pk_bf16_f32 v129, v66, v67\n\t"
        "v_cvt_pk_bf16_f32 v130, v68, v69\n\t"
        "v_cvt_pk_bf16_f32 v131, v70, v71\n\t"
        "v_exp_f32 v72, v72\n\t"
        "v_exp_f32 v73, v73\n\t"
        "v_exp_f32 v74, v74\n\t"
        "s_waitcnt lgkmcnt(3)\n\t"
        "v_mfma_f32_32x32x16_bf16 v[96:111], v[144:147], v[128:131], v[96:111]\n\t"
        "v_exp_f32 v75, v75\n\t"
        "v_exp_f32 v76, v76\n\t"
        "v_exp_f32 v77, v77\n\t"
        "v_exp_f32 v78, v78\n\t"
        "v_exp_f32 v79, v79\n\t"
        "v_cvt_pk_bf16_f32 v132, v72, v73\n\t"
        "v_cvt_pk_bf16_f32 v133, v74, v75\n\t"
        "v_cvt_pk_bf16_f32 v134, v76, v77\n\t"
        "v_cvt_pk_bf16_f32 v135, v78, v79\n\t"
        "s_waitcnt lgkmcnt(2)\n\t"
        "v_mfma_f32_32x32x16_bf16 v[64:79], v[136:139], %[q], v[112:127]\n\t"
        "ds_read_b128 v[140:143], %[ka] offset:5120\n\t"
        "v_exp_f32 v80, v80\n\t"
        "v_exp_f32 v81, v81\n\t"
        "v_exp_f32 v82, v82\n\t"
        "v_exp_f32 v83, v83\n\t"
        "v_exp_f32 v84, v84\n\t"
        "v_exp_f32 v85, v85\n\t"
        "v_mfma_f32_32x32x16_bf16 v[96:111], v[148:151], v[132:135], v[96:111]\n\t"
        "s_mov_b64 exec, %[vm]\n\t"
        "ds_read_b128 v[144:147], %[va] offset:4096\n\t"
        "ds_read_b128 v[148:151], %[va] offset:4608\n\t"
        "s_mov_b64 exec, -1\n\t"
        "v_exp_f32 v86, v86\n\t"
        "v_exp_f32 v87, v87\n\t"
        "v_cvt_pk_bf16_f32 v128, v80, v81\n\t"
        "v_cvt_pk_bf16_f32 v129, v82, v83\n\t"
        "v_cvt_pk_bf16_f32 v130, v84, v85\n\t"
        "v_cvt_pk_bf16_f32 v131, v86, v87\n\t"
        "v_exp_f32 v88, v88\n\t"
        "v_exp_f32 v89, v89\n\t"
        "v_exp_f32 v90, v90\n\t"
        "s_waitcnt lgkmcnt(3)\n\t"
        "v_mfma_f32_32x32x16_bf16 v[96:111], v[152:155], v[128:131], v[96:111]\n\t"
        "v_exp_f32 v91, v91\n\t"
        "v_exp_f32 v92, v92\n\t"
        "v_exp_f32 v93, v93\n\t"
        "v_exp_f32 v94, v94\n\t"
        "v_exp_f32 v95, v95\n\t"
        "v_add_u32 %[ka], 0x1000, %[ka]\n\t"
        "v_add_u32 %[va], 0x1000, %[va]\n\t"
        "v_cvt_pk_bf16_f32 v132, v88, v89\n\t"
        "v_cvt_pk_bf16_f32 v133, v90, v91\n\t"
        "v_cvt_pk_bf16_f32 v134, v92, v93\n\t"
        "v_cvt_pk_bf16_f32 v135, v94, v95\n\t"
        "s_add_u32 %[it], %[it], 1\n\t"
        "s_cmp_lt_u32 %[it], 16\n\t"
        "s_cbranch_scc1 1b\n\t"
        "s_branch 9f\n\t"
        "3:\n\t"
        "s_waitcnt vmcnt(0)\n\t"
        "v_mov_b32 v160, 1\n\t"
        "s_mov_b64 exec, 1\n\t"
        "ds_add_u32 %[cn], v160 offset:4\n\t"
        "s_mov_b64 exec, -1\n\t"
        "s_mov_b32 %[sp], 0\n\t"
        "5:\n\t"
        "ds_read_b32 v160, %[cn] offset:4\n\t"
        "s_waitcnt lgkmcnt(0)\n\t"
        "v_readfirstlane_b32 %[st], v160\n\t"
        "s_add_u32 %[sp], %[sp], 1\n\t"
        "s_cmp_ge_u32 %[st], 8\n\t"
        "s_cbranch_scc1 2b\n\t"
        "s_sleep 1\n\t"
        "s_cmp_lt_u32 %[sp], %[cap]\n\t"
        "s_cbranch_scc1 5b\n\t"
        "s_branch 2b\n\t"
        "4:\n\t"
        "v_mov_b32 v160, 1\n\t"
        "s_mov_b64 exec, 1\n\t"
        "ds_add_u32 %[cn], v160 offset:32\n\t"
        "s_mov_b64 exec, -1\n\t"
        "s_mov_b32 %[sp], 0\n\t"
        "6:\n\t"
        "ds_read_b32 v160, %[cn] offset:8\n\t"
        "s_waitcnt lgkmcnt(0)\n\t"
        "v_readfirstlane_b32 %[st], v160\n\t"
        "s_add_u32 %[sp], %[sp], 1\n\t"
        "s_cmp_ge_u32 %[st], 4\n\t"
        "s_cbranch_scc1 2b\n\t"
        "s_sleep 2\n\t"
        "s_cmp_lt_u32 %[sp], %[cap]\n\t"
        "s_cbranch_scc1 6b\n\t"
        "s_branch 2b\n\t"
        "9:\n\t"
        "s_nop 1\n\t"
        "v_mfma_f32_32x32x16_bf16 v[96:111], v[156:159], v[132:135], v[96:111]\n\t"
        "s_waitcnt lgkmcnt(0)\n\t"
        "s_nop 15\n\t"
        "v_mov_b32 %[o0], v96\n\t"
        "v_mov_b32 %[o1], v97\n\t"
        "v_mov_b32 %[o2], v98\n\t"
        "v_mov_b32 %[o3], v99\n\t"
        "v_mov_b32 %[o4], v100\n\t"
        "v_mov_b32 %[o5], v101\n\t"
        "v_mov_b32 %[o6], v102\n\t"
        "v_mov_b32 %[o7], v103\n\t"
        "v_mov_b32 %[o8], v104\n\t"
        : [o0] "=&v"(o[0]), [o1] "=&v"(o[1]), [o2] "=&v"(o[2]), [o3] "=&v"(o[3]), [o4] "=&v"(o[4]), [o5] "=&v"(o[5]), [o6] "=&v"(o[6]), [o7] "=&v"(o[7]), [o8] "=&v"(o[8]), [ka] "+v"(ka), [va] "+v"(va), [it] "=&s"(s_it), [sp] "=&s"(s_sp), [st] "=&s"(s_st)
        : [q] "v"(qf), [nm] "v"(nm), [one] "v"(onev), [vm] "s"(0x0000ffff0000ffffull), [cn] "v"(cnt_addr), [cap] "s"(SPIN_CAP), [pd] "i"(PRIO_DROP)
        : "memory", "scc", "v64", "v65", "v66", "v67", "v68", "v69", "v70", "v71", "v72", "v73", "v74", "v75", "v76", "v77", "v78", "v79", "v80", "v81", "v82", "v83", "v84", "v85", "v86", "v87", "v88", "v89", "v90", "v91", "v92", "v93", "v94", "v95", "v96", "v97", "v98", "v99", "v100", "v101", "v102", "v103", "v104", "v105", "v106", "v107", "v108", "v109", "v110", "v111", "v112", "v113", "v114", "v115", "v116", "v117", "v118", "v119", "v120", "v121", "v122", "v123", "v124", "v125", "v126", "v127", "v128", "v129", "v130", "v131", "v132", "v133", "v134", "v135", "v136", "v137", "v138", "v139", "v140", "v141", "v142", "v143", "v144", "v145", "v146", "v147", "v148", "v149", "v150", "v151", "v152", "v153", "v154", "v155", "v156", "v157", "v158", "v159", "v160");
        }
        STAMP(4); CSTAMP(11);
        float big = __builtin_fabsf(o[8]);
#pragma unroll
        for (int i = 0; i < 8; ++i) big = __builtin_fmaxf(big, __builtin_fabsf(o[i]) * 0x1p-60f);
        if (__builtin_expect(__any(!(big < 0x1p60f) || !(o[8] > 0x1p-20f)), 0)) {
            float mr = -INFINITY; f32x16 oo = zero;
            const bf16x8 ones = asfrag((u32x4){0x3f803f80u, 0x3f803f80u, 0x3f803f80u, 0x3f803f80u});
#pragma unroll 1
            for (int t = 0; t < 64; ++t) {
                u32x4 kf, v0, v1;
                const unsigned vo = hi * 256 + (lane & 15) * 16;
                BLD_DEV(kf, rws, WS_K + (head * 64 + t) * 1024, lo16); BLD_DEV(v0, rws, WS_V + (head * 64 + t) * 1024, vo); BLD_DEV(v1, rws, WS_V + (head * 64 + t) * 1024, vo + 512);
                asm volatile("s_waitcnt vmcnt(0)" : "+v"(kf), "+v"(v0), "+v"(v1) :: "memory");
                f32x16 s = MFMA(asfrag(kf), qf, zero);
                const float mn = __builtin_fmaxf(mr, halfmax(max16(s)));
                const float al = __builtin_amdgcn_exp2f(mr - mn); mr = mn;
#pragma unroll
                for (int i = 0; i < 16; ++i) { oo[i] *= al; s[i] = __builtin_amdgcn_exp2f(s[i] - mn); }
                oo = MFMA((lane & 16) ? ones : asfrag(v0), asfrag(pack8(s, 0)), oo);
                oo = MFMA((lane & 16) ? ones : asfrag(v1), asfrag(pack8(s, 1)), oo);
            }
#pragma unroll
            for (int i = 0; i < 9; ++i) o[i] = oo[i];
        }
        const float rl = __builtin_amdgcn_rcpf(o[8]);
        gst_plain_dev(rws, WS_O + (tile * 32 + head) * 1024, lo16,
                      (u32x4){min(cvtpk(o[0] * rl, o[1] * rl), ~1u), min(cvtpk(o[2] * rl, o[3] * rl), ~1u), min(cvtpk(o[4] * rl, o[5] * rl), ~1u), min(cvtpk(o[6] * rl, o[7] * rl), ~1u)});
        if (lane == 0) __hip_atomic_fetch_add((LAS unsigned*)(lds + L_MISC + 36 + (w >> 2) * 4), 1u, __ATOMIC_RELAXED, __HIP_MEMORY_SCOPE_WORKGROUP);
        STAMP(5);
        STAMP_FLUSH();
        __builtin_amdgcn_endpgm();
    }
    const int m = w - 8;
    lds_ptr ring = lds + L_B + m * 8192;
    const int cp = (hg >> 1) * 14 + (hg & 1) * 8 + 2 * m;
    const bool hact = m < ((hg & 1) ? 3 : 4);
    const unsigned swz8 = (lane >> 3) * 16, swzc = ((lane & 7) ^ (((lane >> 4) & 1) << 2)) * 16;
    u64 tag;
    f32x16 a00, a01, a10, a11;
    const u32x4 rw1 = srd(w1 + (hact ? cp : 0) * 64, 512 * 3584 - (hact ? cp : 0) * 64);
    const unsigned w1v0 = (lane >> 3) * 3584 + swzc, w1v1 = w1v0 + 8 * 3584 - 1024;
    auto hslot = [&](int k) -> unsigned { const int s = k % 5; return s < 3 ? L_B + m * 8192 + s * 2048 : (s == 3 ? L_K : L_V) + 24576 + m * 2048; };
    auto w1dma = [&](int k) { dma2(rw1, (((((k >> 2) + rot) & 7) << 2) + (((k & 3) + m) & 3)) * 57344, w1v0, w1v1, l0 + hslot(k)); };
    {
        const u32x4 rx = srd(x + (size_t)(64 * tg + 16 * m) * 1024, 16384), rwq = srd(wqkv + (size_t)(16 * m) * 3072 + hg * 128, 512 * 3072 - (16 * m) * 3072 - hg * 128);
        const unsigned xv0 = (lane >> 3) * 1024 + (((lane & 7) ^ (lane >> 4)) * 16), xv1 = (xv0 ^ 64) + 8192 - 1024;
        const unsigned wv0 = (lane >> 3) * 3072 + swzc, wv1 = wv0 + 8 * 3072 - 1024;
        auto issue = [&](int i) { const int kc = (i + rot) & 7;
            dma2(rx, kc * 128, xv0, xv1, l0 + ximg(kc) + m * 2048);
#pragma unroll
            for (int sg = 0; sg < 3; ++sg) dma2(rwq, kc * (64 * 3072) + sg * 1024, wv0, wv1, l0 + wimg((i & 3) * 3 + sg) + m * 2048); };
        issue(0); issue(1);
        u64 ep; u32x2 bv[8];
        constexpr int WCNT[8] = {8, 16, 25, 25, 25, 16, 8, 7};
#pragma unroll
        for (int i = 0; i < 8; ++i) {
            if (i == 5) asm volatile("s_waitcnt vmcnt(16)" : "+v"(ep), "+v"(bv[0]), "+v"(bv[1]), "+v"(bv[2]), "+v"(bv[3]), "+v"(bv[4]), "+v"(bv[5]), "+v"(bv[6]), "+v"(bv[7]) :: "memory");
            else WAITV(WCNT[i]);
            BARRIER();
            if (i == 0) { issue(2); issue(3); }
            if (i >= 1 && i <= 4) issue(i + 3);
            if (i == 1) {
                asm volatile("global_load_dwordx2 %0, %1, %2 sc1" : "=v"(ep) : "v"(0u), "s"(ws + F_EPOCH) : "memory");
#pragma unroll
                for (int g = 0; g < 8; ++g) asm volatile("global_load_dwordx2 %0, %1, %2" : "=v"(bv[g]) : "v"(hi * 8), "s"(b1 + (hact ? cp : 0) * 16 + 4 * g) : "memory");
            }
            if (i == 6) {
                dma1(srd(gw, 1024), 0, lo16, l0 + L_B + m * 8192 + 7168); w1dma(0); w1dma(1); w1dma(2);
                if (m == 0 && lane == 0) { *(LAS u32x4*)(lds + L_MISC) = (u32x4){0u, 0u, 0u, 0u}; *(LAS u64*)(lds + L_MISC + 16) = ep + 1; *(LAS u64*)(lds + L_MISC + 24) = 0; *(LAS u32x4*)(lds + L_MISC + 32) = (u32x4){0u, 0u, 0u, 0u}; *(LAS u64*)(lds + L_MISC + 48) = 0; }
            }
            if (i == 7) w1dma(3);
        }
        tag = ep + 1;
#pragma unroll
        for (int g = 0; g < 4; ++g)
#pragma unroll
            for (int j = 0; j < 4; ++j) {
                const unsigned u0 = bv[g][j >> 1], u1 = bv[4 + g][j >> 1];
                a00[4 * g + j] = __uint_as_float((j & 1) ? (u0 & 0xffff0000u) : (u0 << 16));
                a10[4 * g + j] = __uint_as_float((j & 1) ? (u1 & 0xffff0000u) : (u1 << 16));
            }
        a01 = a00; a11 = a10;
        BARRIER();
    }
    const u32x4 rws = srd(ws, WS_BYTES);
    {
        __builtin_amdgcn_s_setprio(2);
        if (HGATE == 1) lds_wait(lds + L_MISC + 24, 8);
        if (HGATE == 2) lds_wait(lds + L_MISC + 28, 1);
        if (HGATE == 3) lds_wait(lds + L_MISC, 8);
        const unsigned trn = (8 * hi + ((lane & 15) >> 2)) * 128 + (((lane >> 3) & 1) * 64) + ((lane >> 4) & 1) * 32 + (lane & 3) * 8;
        bf16x8 f0[2], f1[2], xa[2], xc[2];
        float pA = 0.f, pB = 0.f;
        u32x4 gv;
        unsigned xs[4];
#pragma unroll
        for (int j = 0; j < 4; ++j) xs[j] = xfo ^ (((j + m) & 3) * 32);
#pragma unroll
        for (int k = 0; k <= 32; ++k) {
            if ((k & 3) == 0 && k < 32) STAMP(8 + (k >> 2));
            lds_ptr sl = lds + hslot(k);
            unsigned xb = 0;
            if (k < 32) {
                WAITV(k + 4 <= 32 ? 6 : 2 * (31 - k));
                xb = ximg(((k >> 2) + rot) & 7) + xs[k & 3];
            }
            if (k > 0) a00 = MFMA(f0[(k - 1) & 1], xa[(k - 1) & 1], a00);
            if (k < 32) { xa[k & 1] = ldfrag(lds + xb); f0[k & 1] = trfrag(sl + trn, 512); }
            __builtin_amdgcn_sched_barrier(0);
            if (k > 0) a10 = MFMA(f1[(k - 1) & 1], xa[(k - 1) & 1], a10);
            if (k < 32) { f1[k & 1] = trfrag(sl + (trn ^ 64), 512); xc[k & 1] = ldfrag(lds + xb + 4096); }
            __builtin_amdgcn_sched_barrier(0);
            if (k > 0) a01 = MFMA(f0[(k - 1) & 1], xc[(k - 1) & 1], a01);
            __builtin_amdgcn_sched_barrier(0);
            if (k + 4 < 32) w1dma(k + 4);
            if (k > 0) a11 = MFMA(f1[(k - 1) & 1], xc[(k - 1) & 1], a11);
            if ((k & 3) == 1) {
                const u32x4 ua = __builtin_bit_cast(u32x4, xa[(k - 1) & 1]), uc = __builtin_bit_cast(u32x4, xc[(k - 1) & 1]);
#pragma unroll
                for (int j = 0; j < 4; ++j) {
                    const unsigned a_ = ua[j], c_ = uc[j], g_ = gv[j];
                    pA = __builtin_amdgcn_fdot2_f32_bf16(__builtin_bit_cast(bf16x2, a_), __builtin_bit_cast(bf16x2, g_), pA, false);
                    pB = __builtin_amdgcn_fdot2_f32_bf16(__builtin_bit_cast(bf16x2, c_), __builtin_bit_cast(bf16x2, g_), pB, false);
                }
            }
            if (k < 32 && (k & 3) == 0) gv = *(LAS const u32x4*)(ring + 7168 + ((((k >> 2) + rot) & 7) * 64 + 16 * m + 8 * hi) * 2);
            __builtin_amdgcn_sched_barrier(0);
        }
        asm volatile("" : "+v"(a00), "+v"(a01), "+v"(a10), "+v"(a11));
        *(LAS float*)(ring + 6144 + 512 + lane * 4) = pA; *(LAS float*)(ring + 6144 + 768 + lane * 4) = pB;
    }
    STAMP(1);
    lds_arrive(lds + L_MISC + 12, lane);
    lds_wait(lds + L_MISC + 28, 1);
    dma2_dev(rws, WS_K + (head * 64 + 24 + 2 * m) * 1024, lo16, l0 + L_K + (24 + 2 * m) * 1024);
    dma2_dev(rws, WS_V + (head * 64 + 24 + 2 * m) * 1024, lo16, l0 + L_V + (24 + 2 * m) * 1024);
    lds_wait(lds + L_MISC + 12, 4);
#pragma unroll
    for (int j = 0; j < 2; ++j) {
        dma4_dev(rws, WS_K + (head * 64 + 32 + 16 * j + 4 * m) * 1024, lo16, l0 + L_K + (32 + 16 * j + 4 * m) * 1024);
        dma4_dev(rws, WS_V + (head * 64 + 32 + 16 * j + 4 * m) * 1024, lo16, l0 + L_V + (32 + 16 * j + 4 * m) * 1024);
    }
    {
        const u32x4 ones = {~0u, ~0u, ~0u, ~0u};
        const unsigned oa = WS_O + ((8 * (bi >> 2) + 2 * m) * 32 + head) * 1024;
        gst_plain_dev(rws, oa, lo16, ones); gst_plain_dev(rws, oa + 32768, lo16, ones);
    }
    float zA = 0.f, zB = 0.f;
#pragma unroll
    for (int q = 0; q < 4; ++q) {
        lds_ptr pp = lds + L_B + q * 8192 + 6144 + 512 + (lane & 31) * 4;
        zA += *(LAS const float*)pp + *(LAS const float*)(pp + 128); zB += *(LAS const float*)(pp + 256) + *(LAS const float*)(pp + 384);
    }
    asm volatile("" : "+v"(zA), "+v"(zB));
    {
        const unsigned hd = WS_H + (2 * tg * 112 + 2 * cp) * 1024;
        u32x4 h0, h1, h2, h3;
        if (hact) { h0 = mish8(a00, 0); h1 = mish8(a00, 1); h2 = mish8(a10, 0); h3 = mish8(a10, 1); asm volatile("" : "+v"(h0), "+v"(h1), "+v"(h2), "+v"(h3)); }
        WAITV(0); lds_arrive(lds + L_MISC + 8, lane);
        if (hact) {
            gst_plain_dev(rws, hd, lo16, h0); gst_plain_dev(rws, hd + 1024, lo16, h1); gst_plain_dev(rws, hd + 2048, lo16, h2); gst_plain_dev(rws, hd + 3072, lo16, h3);
            gst_plain_dev(rws, hd + 112 * 1024, lo16, mish8(a01, 0)); gst_plain_dev(rws, hd + 113 * 1024, lo16, mish8(a01, 1));
            gst_plain_dev(rws, hd + 114 * 1024, lo16, mish8(a11, 0)); gst_plain_dev(rws, hd + 115 * 1024, lo16, mish8(a11, 1));
        }
    }
    publish(ws, F_H + (tg * 32 + hg * 4 + m) * FS, tag, lane);
    __builtin_amdgcn_s_setprio(0);
    STAMP(2);
    u32x4 st[16];
    const unsigned stw = swz8 * 8 + swzc;
    const unsigned w2v0 = (lane >> 3) * 1024 + (lane & 7) * 16, w2v1 = w2v0 + 8192, hv1 = lo16 + 112 * 1024;
    const unsigned trp = (4 * hi + ((lane & 15) >> 2)) * 128 + (((lane >> 3) & 1) * 64) + ((lane >> 4) & 1) * 32 + (lane & 3) * 8;
    {
        const u32x4 rw2 = srd(w2 + (size_t)(28 * m) * 16384 + hg * 128, 28 * 16384 - hg * 128);
        const unsigned hs = WS_H + (2 * tg * 112 + 28 * m) * 1024;
#pragma unroll
        for (int k = 0; k < 4; ++k) { BLD(st[2 * k], rw2, k * 16384, w2v0); BLD(st[2 * k + 1], rw2, k * 16384, w2v1); }
        {
            const int hh = 8 * m + (lane & 7), pb = ((tg >> 2) << 2) | (hh & 3), px = hh >> 2;
            const int ptg = (px >> 1) * 8 + (pb & 7), phg = (px & 1) * 4 + (pb >> 3);
            const unsigned idx = lane < 32 ? tg * 32 + lane : ptg * 32 + phg * 4 + (tg & 3);
            poll_at(ws + F_H + idx * FS, tag);
        }
        STAMP(3);
#pragma unroll
        for (int k = 0; k < 4; ++k) { BLD_DEV(st[8 + 2 * k], rws, hs + k * 1024, lo16); BLD_DEV(st[9 + 2 * k], rws, hs + k * 1024, hv1); }
        WAITV(0);
        auto bsteps = [&](int it, auto LAST, auto FIRST) {
#pragma unroll
            for (int k = 0; k < 4; ++k) {
                asm volatile("s_waitcnt vmcnt(%4)" : "+v"(st[2 * k]), "+v"(st[2 * k + 1]), "+v"(st[8 + 2 * k]), "+v"(st[9 + 2 * k]) : "i"(LAST.value ? 12 - 4 * k : 12) : "memory");
                lds_ptr sl = ring + (k & 1) * 2048;
                *(LAS u32x4*)(sl + stw) = st[2 * k]; *(LAS u32x4*)(sl + 1024 + stw) = st[2 * k + 1];
                const bf16x8 f0 = trfrag(sl + trp, 1024), f1 = trfrag(sl + (trp ^ 64), 1024);
                const bf16x8 xa = asfrag(st[8 + 2 * k]), xc = asfrag(st[9 + 2 * k]);
                if (FIRST.value && k == 0) { const f32x16 z = {}; a00 = MFMA(f0, xa, z); a01 = MFMA(f0, xc, z); a10 = MFMA(f1, xa, z); a11 = MFMA(f1, xc, z); }
                else { a00 = MFMA(f0, xa, a00); a01 = MFMA(f0, xc, a01); a10 = MFMA(f1, xa, a10); a11 = MFMA(f1, xc, a11); }
                if (!LAST.value) { const int kn = 4 * (it + 1) + k;
                    BLD(st[2 * k], rw2, kn * 16384, w2v0); BLD(st[2 * k + 1], rw2, kn * 16384, w2v1);
                    BLD_DEV(st[8 + 2 * k], rws, hs + kn * 1024, lo16); BLD_DEV(st[9 + 2 * k], rws, hs + kn * 1024, hv1); }
            } };
        bsteps(0, std::false_type{}, std::true_type{});
#pragma unroll 1
        for (int it = 1; it < 6; ++it) bsteps(it, std::false_type{}, std::false_type{});
        bsteps(6, std::true_type{}, std::false_type{});
        asm volatile("" : "+v"(a00), "+v"(a01), "+v"(a10), "+v"(a11));
    }
    STAMP(4);
    __builtin_amdgcn_s_setprio(2);
    zA = __builtin_fmaxf(zA, -30.f); zB = __builtin_fmaxf(zB, -30.f);
    const float rhoA = __builtin_amdgcn_exp2f(-zA * LOG2E), rhoB = __builtin_amdgcn_exp2f(-zB * LOG2E);
    bf16x8 wf[10];
    {
        const u32x4 rwo = srd(wo + (size_t)(8 * m) * 16384 + hg * 128, 8 * 16384 - hg * 128);
#pragma unroll
        for (int k = 0; k < 8; ++k) { BLD(st[2 * k], rwo, k * 16384, w2v0); BLD(st[2 * k + 1], rwo, k * 16384, w2v1); }
#pragma unroll
        for (int i = 0; i < 16; ++i) { a00[i] *= rhoA; a10[i] *= rhoA; a01[i] *= rhoB; a11[i] *= rhoB; }
        asm volatile("" : "+v"(a00), "+v"(a01), "+v"(a10), "+v"(a11));
        __builtin_amdgcn_s_setprio(0);
#pragma unroll
        for (int k = 0; k < 8; ++k) {
            asm volatile("s_waitcnt vmcnt(%2)" : "+v"(st[2 * k]), "+v"(st[2 * k + 1]) : "i"(14 - 2 * k) : "memory");
            lds_ptr sl = ring + (k < 5 ? (k & 1) : (k - 5)) * 2048;
            *(LAS u32x4*)(sl + stw) = st[2 * k]; *(LAS u32x4*)(sl + 1024 + stw) = st[2 * k + 1];
            if (k < 5) { wf[2 * k] = trfrag(sl + trp, 1024); wf[2 * k + 1] = trfrag(sl + (trp ^ 64), 1024); }
        }
    }
    const float gA = __builtin_amdgcn_rcpf(1.f + rhoA), gB = __builtin_amdgcn_rcpf(1.f + rhoB);
    lds_wait(lds + L_MISC + 32, 8);
#pragma unroll
    for (int tt = 0; tt < 2; ++tt) {
        lds_wait_slow(lds + L_MISC + 36 + tt * 4, 4);
        __builtin_amdgcn_s_setprio(TAILPRIO);
        const unsigned os = WS_O + ((2 * tg + tt) * 32 + 8 * m) * 1024;
#pragma unroll
        for (int k = 0; k < 8; ++k) BLD_DEV(st[k], rws, os + k * 1024, lo16);
        f32x16& c0 = tt ? a01 : a00; f32x16& c1 = tt ? a11 : a10;
#pragma unroll
        for (int k = 0; k < 8; ++k) {
            bf16x8 f0, f1;
            if (k < 5) { f0 = wf[2 * k]; f1 = wf[2 * k + 1]; } else { f0 = trfrag(ring + (k - 5) * 2048 + trp, 1024); f1 = trfrag(ring + (k - 5) * 2048 + (trp ^ 64), 1024); }
            asm volatile("s_waitcnt vmcnt(%1)" : "+v"(st[k]) : "i"(7 - k) : "memory");
#pragma unroll 1
            for (unsigned sp = 0; sp < SPIN_CAP && __any(max(max(st[k][0], st[k][1]), max(st[k][2], st[k][3])) == ~0u); ++sp) {
                __builtin_amdgcn_s_sleep(RETRY_SLEEP);
                BLD_DEV(st[k], rws, os + k * 1024, lo16);
                asm volatile("s_waitcnt vmcnt(0)" : "+v"(st[k]) :: "memory");
            }
            c0 = MFMA(f0, asfrag(st[k]), c0); c1 = MFMA(f1, asfrag(st[k]), c1);
        }
        STAMP(5 + tt);
        auto xarea = [&](int d) -> lds_ptr { return lds + (tt ? L_K + 32768 + d * 8192 : (d < 2 ? L_K : L_V) + (d & 1) * 8192) + lo16; };
#pragma unroll
        for (int d = 0; d < 4; ++d) {
            const f32x16& a = (d >> 1) ? c1 : c0;
#pragma unroll
            for (int q = 0; q < 2; ++q) *(LAS f32x4*)(xarea(d) + m * 2048 + q * 1024) = (f32x4){a[8 * (d & 1) + 4 * q], a[8 * (d & 1) + 4 * q + 1], a[8 * (d & 1) + 4 * q + 2], a[8 * (d & 1) + 4 * q + 3]};
        }
        lds_arrive(lds + L_MISC + 48 + tt * 4, lane); lds_wait(lds + L_MISC + 48 + tt * 4, 4);
        f32x4 s0 = *(LAS const f32x4*)xarea(m), s1 = *(LAS const f32x4*)(xarea(m) + 1024);
#pragma unroll
        for (int o = 1; o < 4; ++o) { s0 += *(LAS const f32x4*)(xarea(m) + o * 2048); s1 += *(LAS const f32x4*)(xarea(m) + o * 2048 + 1024); }
        const float r[8] = {s0[0], s0[1], s0[2], s0[3], s1[0], s1[1], s1[2], s1[3]};
        const float g = tt ? gB : gA;
        const unsigned a0 = cvtpk(r[0] * g, r[1] * g), a1 = cvtpk(r[2] * g, r[3] * g), b0 = cvtpk(r[4] * g, r[5] * g), b1v = cvtpk(r[6] * g, r[7] * g);
        auto r0 = __builtin_amdgcn_permlane32_swap(a0, b0, false, false);
        auto r1 = __builtin_amdgcn_permlane32_swap(a1, b1v, false, false);
        *(u32x4*)(out + (size_t)(64 * tg + tt * 32 + (lane & 31)) * 1024 + hg * 128 + m * 32 + hi * 16) = (u32x4){r0[0], r1[0], r0[1], r1[1]};
        if (tt == 0) __builtin_amdgcn_s_setprio(0);
    }
    if (b == 0 && tid == 512) __hip_atomic_store((GAS u64*)(ws + F_EPOCH), tag, __ATOMIC_RELAXED, __HIP_MEMORY_SCOPE_AGENT);
    STAMP(7);
    STAMP_FLUSH();
}

extern "C" void kernel_launch(void* const* d_in, const int* in_sizes, int n_in, void* d_out, int out_size, void* d_ws, size_t ws_size, hipStream_t stream) {
    (void)hipFuncSetAttribute((const void*)fused_kernel, hipFuncAttributeMaxDynamicSharedMemorySize, LDS_BYTES);
    fused_kernel<<<256, 768, LDS_BYTES, stream>>>((const char*)d_in[0], (const char*)d_in[1], (const char*)d_in[2], (const char*)d_in[4], (const unsigned*)d_in[5],
                                                  (const char*)d_in[6], (const char*)d_in[7], (char*)d_ws, (char*)d_out);
}
```
